```python
import torch

def _softmax(x, axis=-1):
    return torch.softmax(x, dim=axis)
def _gelu(x):
    return torch.nn.functional.gelu(x, approximate="tanh")
def _silu(x):
    return torch.nn.functional.silu(x)
def _sigmoid(x):
    return torch.sigmoid(x)
def _layernorm(x, axis=-1, eps=1e-5):
    m = x.mean(dim=axis, keepdim=True)
    v = x.var(dim=axis, keepdim=True, unbiased=False)
    return (x - m) / torch.sqrt(v + eps)
def _scatter_add(tgt, idx, src, axis=0):
    return tgt.clone().index_add_(axis, idx, src)
def _segment_sum(src, seg, n):
    out = torch.zeros((n,) + src.shape[1:], dtype=src.dtype)
    return out.index_add_(0, seg, src)
def _pad(x, pw):
    flat = tuple(v for pair in reversed(pw) for v in pair)
    return torch.nn.functional.pad(x, flat)

kernel_name = 'standalone__gqa_softmax__near_ridge'

def setup_inputs():

    _w = (lambda a: (lambda t: t.to(torch.bfloat16) if t.is_floating_point() else t)(torch.as_tensor(a)))
    B, Hq, Hkv, Sq, Sk, D = 8, 32, 8, 48, 2048, 128
    return {
        "q": _w((lambda *s: torch.randn(*s, dtype=torch.bfloat16) * 0.8)(B, Hq, Sq, D)),
        "k": _w((lambda *s: torch.randn(*s, dtype=torch.bfloat16) * 0.8)(B, Hkv, Sk, D)),
        "v": _w((lambda *s: torch.randn(*s, dtype=torch.bfloat16) * 0.05)(B, Hkv, Sk, D)),
    }

def reference(q, k, v):
    B, Hq, Sq, D = q.shape
    Hkv = k.shape[1]
    rep = Hq // Hkv
    k = (lambda x, n, axis: torch.repeat_interleave(x, n, dim=axis))(k, rep, axis=1)
    v = (lambda x, n, axis: torch.repeat_interleave(x, n, dim=axis))(v, rep, axis=1)
    scale = D ** -0.5
    scores = torch.einsum("bhqd,bhkd->bhqk", q, k) * scale
    p = _softmax(scores, axis=-1)
    return torch.einsum("bhqk,bhkd->bhqd", p, v)
```

```cpp
#include <hip/hip_runtime.h>
#include <hip/hip_bf16.h>
#include <cstdio>

#define ATTN_B 8
#define ATTN_HQ 32
#define ATTN_HKV 8
#define ATTN_SQ 48
#define ATTN_SK 2048
#define ATTN_NSPLIT 4
#define ATTN_THR 8
#define ATTN_KVCONV 0
#define ATTN_CAUSAL 0
#define ATTN_LENS 0
#define ATTN_QPARK (4 + ATTN_LENS)
#define ATTN_WG4 1
#define ATTN_QPARK4 4
#define ATTN_XCHG4 1
constexpr int B = ATTN_B, HQ = ATTN_HQ, HKV = ATTN_HKV, SQ = ATTN_SQ, SK = ATTN_SK;
constexpr int NSPLIT = ATTN_NSPLIT;
constexpr float THR = ATTN_THR;
constexpr int CAUSAL = ATTN_CAUSAL;
constexpr int HAS_LENS = ATTN_LENS;
constexpr int KV_CONVERT = ATTN_KVCONV;
constexpr int QPARK = ATTN_QPARK;
constexpr int WG4 = ATTN_WG4;
constexpr int XCHG4 = ATTN_XCHG4;
constexpr int QPARK4 = ATTN_QPARK4;
constexpr int D = 128, NW = 8, QBLK = 32, KVBLK = 64;
constexpr float SCALE = 0.088388347648318447f;
constexpr int G = HQ / HKV, ROWS = G * SQ, NT = SK / KVBLK, TPS = NT / NSPLIT;
constexpr int NWB = (WG4 && ROWS <= QBLK * 4 && !CAUSAL && !HAS_LENS && TPS % 4 == 0) ? 4 : NW, NSB = NSPLIT * (NW / NWB), TPSB = NT / NSB;
static_assert(HQ % HKV == 0 && ROWS >= 1 && ROWS <= QBLK * NW, "G*SQ must fit the workgroup's 256 MFMA rows (larger SQ -> dense body)");
static_assert(!(CAUSAL && HAS_LENS && !KV_CONVERT), "CAUSAL + HAS_LENS on the f32 convert-on-load loop spills 2 VGPRs (ROCm 7.2, QPARK 4..8): set KV_CONVERT 1 (bf16 loop, 0 spill) for that combination");
static_assert(SK % KVBLK == 0 && NT % NSPLIT == 0 && TPS >= 2 && TPS % 2 == 0, "SK must give every split an even number (>= 2) of 64-key tiles");
static_assert(NT % NSB == 0 && TPSB >= 2 && TPSB % 2 == 0, "internal: half-split tile count (NWB falls back to 8 waves when TPS % 4 != 0)");
constexpr float L2E = 1.4426950408889634f, SL2E = SCALE * L2E, THR2 = THR * L2E;
constexpr size_t SHM_V = KVBLK * D * 2, SHM_K = KVBLK * D * 2, SHM_WS = 2 * SHM_V + 2 * SHM_K;
template <int NWT> constexpr size_t shm_qp() { return SHM_WS + ((NWT == NW || !XCHG4) ? (size_t)NWT * 64 * 4 : 0); }
static_assert(QPARK >= 0 && QPARK <= 8 && QPARK4 >= 0 && QPARK4 <= 8, "QPARK");
constexpr size_t WS_PML = 4096, PML_BYTES = (size_t)B * HQ * SQ * NSB * 2 * 4;
constexpr size_t WS_PO = (WS_PML + PML_BYTES + 255) / 256 * 256, WS_PEND = NSB > 1 ? WS_PO + (size_t)B * HQ * SQ * NSB * D * 4 : WS_PML;
constexpr size_t NQ = (size_t)B * HQ * SQ * D, NKV = (size_t)B * HKV * SK * D;
constexpr size_t WS_QB = (WS_PEND + 255) / 256 * 256, WS_KB = WS_QB + NQ * 2, WS_VB = WS_KB + NKV * 2;
constexpr size_t WS_END = KV_CONVERT ? WS_VB + NKV * 2 : WS_PEND;

using bf16 = __hip_bfloat16;
using bf16x8 = __attribute__((ext_vector_type(8))) short;
using s16x4 = __attribute__((ext_vector_type(4))) short;
using f32x16 = __attribute__((ext_vector_type(16))) float;
using f32x4 = __attribute__((ext_vector_type(4))) float;
using u32x4 = __attribute__((ext_vector_type(4))) unsigned;
template <class T> struct InCfg;
template <> struct InCfg<__hip_bfloat16> { static constexpr int NWT = NWB, QP = NWB == 4 ? QPARK4 : 0; };
template <> struct InCfg<float> { static constexpr int NWT = NW, QP = QPARK; };
template <class T> constexpr int nw_for() { return InCfg<T>::NWT; }
template <class T> constexpr int ns_for() { return NSPLIT * (NW / InCfg<T>::NWT); }
template <class T> constexpr size_t shm_bytes() { return shm_qp<InCfg<T>::NWT>() + (size_t)InCfg<T>::QP * 64 * InCfg<T>::NWT * 16; }
#define KSWZ(row, colB) ((row) * 256 + ((colB) ^ (((row) & 7) << 4)))
#define SBAR() __builtin_amdgcn_sched_barrier(0)
__device__ __forceinline__ int crow(int r, int hi) { return (r & 3) + 8 * (r >> 2) + 4 * hi; }
__device__ __forceinline__ unsigned cvtpk(float lo, float hi) { unsigned r; asm volatile("v_cvt_pk_bf16_f32 %0, %1, %2" : "=v"(r) : "v"(lo), "v"(hi)); return r; }
__device__ __forceinline__ bf16x8 pack8(f32x4 a, f32x4 b) { u32x4 w = {cvtpk(a[0], a[1]), cvtpk(a[2], a[3]), cvtpk(b[0], b[1]), cvtpk(b[2], b[3])}; return *reinterpret_cast<bf16x8*>(&w); }
__device__ __forceinline__ bf16x8 ld8(const bf16* p) { return *reinterpret_cast<const bf16x8*>(p); }
__device__ __forceinline__ bf16x8 ld8(const float* p) { return pack8(*reinterpret_cast<const f32x4*>(p), *reinterpret_cast<const f32x4*>(p + 4)); }
template <class T, int NR> struct Stage;
template <> struct Stage<bf16, 2> { bf16x8 v0, v1, k0, k1;
  __device__ __forceinline__ void load(const bf16* V0, const bf16* V1, const bf16* K0, const bf16* K1) { v0 = ld8(V0); v1 = ld8(V1); k0 = ld8(K0); k1 = ld8(K1); }
  __device__ __forceinline__ bf16x8 gv0() const { return v0; } __device__ __forceinline__ bf16x8 gv1() const { return v1; }
  __device__ __forceinline__ bf16x8 gk0() const { return k0; } __device__ __forceinline__ bf16x8 gk1() const { return k1; } };
template <> struct Stage<float, 2> { f32x4 v0a, v0b, v1a, v1b, k0a, k0b, k1a, k1b;
  __device__ __forceinline__ void load(const float* V0, const float* V1, const float* K0, const float* K1) {
    v0a = *(const f32x4*)V0; v0b = *(const f32x4*)(V0 + 4); v1a = *(const f32x4*)V1; v1b = *(const f32x4*)(V1 + 4);
    k0a = *(const f32x4*)K0; k0b = *(const f32x4*)(K0 + 4); k1a = *(const f32x4*)K1; k1b = *(const f32x4*)(K1 + 4); }
  __device__ __forceinline__ bf16x8 gv0() const { return pack8(v0a, v0b); } __device__ __forceinline__ bf16x8 gv1() const { return pack8(v1a, v1b); }
  __device__ __forceinline__ bf16x8 gk0() const { return pack8(k0a, k0b); } __device__ __forceinline__ bf16x8 gk1() const { return pack8(k1a, k1b); } };
template <int NR> struct Stage<bf16, NR> { bf16x8 v[NR], k[NR];
  __device__ __forceinline__ void ldv(int i, const bf16* p) { v[i] = ld8(p); } __device__ __forceinline__ void ldk(int i, const bf16* p) { k[i] = ld8(p); }
  __device__ __forceinline__ bf16x8 gv(int i) const { return v[i]; } __device__ __forceinline__ bf16x8 gk(int i) const { return k[i]; } };
template <int NR> struct Stage<float, NR> { f32x4 va[NR], vb[NR], ka[NR], kb[NR];
  __device__ __forceinline__ void ldv(int i, const float* p) { va[i] = *(const f32x4*)p; vb[i] = *(const f32x4*)(p + 4); }
  __device__ __forceinline__ void ldk(int i, const float* p) { ka[i] = *(const f32x4*)p; kb[i] = *(const f32x4*)(p + 4); }
  __device__ __forceinline__ bf16x8 gv(int i) const { return pack8(va[i], vb[i]); } __device__ __forceinline__ bf16x8 gk(int i) const { return pack8(ka[i], kb[i]); } };
__device__ __forceinline__ int voff(int row, int sc) { return (row >> 5) * 8192 + 2 * ((sc >> 4) * 512 + (row & 31) * 16 + (sc & 8)); }

__device__ __forceinline__ void partialSM(f32x16& p0, f32x16& p1, float& m_reg, float& msub, float& alpha, int kbase, int lim, int lim0) {
  if constexpr (CAUSAL || HAS_LENS) { if (kbase + KVBLK - 1 > lim0) {
    const float ninf = -__builtin_inff(); const int hi = (threadIdx.x & 63) >> 5;
    for (int r = 0; r < 16; ++r) { const int k = kbase + crow(r, hi); p0[r] = k > lim ? ninf : p0[r]; p1[r] = k + 32 > lim ? ninf : p1[r]; } } }
  float pmax = p0[0]; for (int r = 1; r < 16; ++r) pmax = fmaxf(pmax, p0[r]); for (int r = 0; r < 16; ++r) pmax = fmaxf(pmax, p1[r]);
  { auto rr = __builtin_amdgcn_permlane32_swap(__float_as_uint(pmax), __float_as_uint(pmax), false, false);
    pmax = fmaxf(__uint_as_float(rr[0]), __uint_as_float(rr[1])); }
  pmax *= SL2E;
  if (__builtin_expect(__all(pmax - m_reg <= THR2), 1)) { alpha = 1.f; }
  else { float mn = fmaxf(m_reg, pmax); msub = (mn == -__builtin_inff()) ? 0.f : mn; alpha = __builtin_amdgcn_exp2f(m_reg - msub); m_reg = mn; }
  float nm = -msub;
  for (int r = 0; r < 16; ++r) p0[r] = fmaf(p0[r], SL2E, nm); for (int r = 0; r < 16; ++r) p1[r] = fmaf(p1[r], SL2E, nm);
  for (int r = 0; r < 16; ++r) p0[r] = __builtin_amdgcn_exp2f(p0[r]);
}
__device__ __forceinline__ void finishSM(f32x16& p0, f32x16& p1, float alpha, float& l_reg, bf16x8& pa0, bf16x8& pa1, bf16x8& pa2, bf16x8& pa3) {
  for (int r = 0; r < 16; ++r) p1[r] = __builtin_amdgcn_exp2f(p1[r]);
  float ps = 0; for (int r = 0; r < 16; ++r) ps += p0[r]; for (int r = 0; r < 16; ++r) ps += p1[r];
  { auto rr = __builtin_amdgcn_permlane32_swap(__float_as_uint(ps), __float_as_uint(ps), false, false); ps = __uint_as_float(rr[0]) + __uint_as_float(rr[1]); }
  l_reg = l_reg * alpha + ps;
#define PK4(P, BASE, OUT) do { unsigned a0 = cvtpk(P[BASE + 0], P[BASE + 1]), a1 = cvtpk(P[BASE + 2], P[BASE + 3]); \
    unsigned b0 = cvtpk(P[BASE + 4], P[BASE + 5]), b1 = cvtpk(P[BASE + 6], P[BASE + 7]); \
    auto r0 = __builtin_amdgcn_permlane32_swap(a0, b0, false, false); auto r1 = __builtin_amdgcn_permlane32_swap(a1, b1, false, false); \
    u32x4 w = {r0[0], r1[0], r0[1], r1[1]}; OUT = *reinterpret_cast<bf16x8*>(&w); } while (0)
  PK4(p0, 0, pa0); PK4(p0, 8, pa1); PK4(p1, 0, pa2); PK4(p1, 8, pa3);
#undef PK4
}
template <int QP, int NWT> __device__ __forceinline__ void qkt(f32x16& p0, f32x16& p1, const bf16* Ks, const bf16x8* qr, const char* qp, int r32, int hi) {
  p0 = f32x16{}; p1 = f32x16{};
#pragma unroll
  for (int d0 = 0; d0 < 8; ++d0) { int cb = (d0 * 16 + hi * 8) * 2;
    bf16x8 b0 = *reinterpret_cast<const bf16x8*>((const char*)Ks + KSWZ(r32, cb));
    bf16x8 b1 = *reinterpret_cast<const bf16x8*>((const char*)Ks + KSWZ(32 + r32, cb));
    const bf16x8 q = d0 < 8 - QP ? qr[d0] : *reinterpret_cast<const bf16x8*>(qp + (d0 - (8 - QP)) * (64 * NWT * 16));
    p0 = __builtin_amdgcn_mfma_f32_32x32x16_bf16(b0, q, p0, 0, 0, 0);
    p1 = __builtin_amdgcn_mfma_f32_32x32x16_bf16(b1, q, p1, 0, 0, 0); }
}
__device__ __forceinline__ void pv_d0(f32x16* o, int vb, bf16x8 pa0, bf16x8 pa1, bf16x8 pa2, bf16x8 pa3) {
#pragma unroll
  for (int d0 = 0; d0 < 4; ++d0) { s16x4 lo[4], hi[4];
#pragma unroll
    for (int ks = 0; ks < 4; ++ks) { int a = vb + d0 * 2048 + (ks >> 1) * 8192 + (ks & 1) * 512;
      asm volatile("ds_read_b64_tr_b16 %0,%1" : "=&v"(lo[ks]) : "v"(a) : "memory");
      asm volatile("ds_read_b64_tr_b16 %0,%1 offset:128" : "=&v"(hi[ks]) : "v"(a) : "memory"); }
    asm volatile("s_waitcnt lgkmcnt(0)" ::: "memory"); SBAR();
#define PK(k) (bf16x8){lo[k][0], lo[k][1], lo[k][2], lo[k][3], hi[k][0], hi[k][1], hi[k][2], hi[k][3]}
    o[d0] = __builtin_amdgcn_mfma_f32_32x32x16_bf16(pa0, PK(0), o[d0], 0, 0, 0);
    o[d0] = __builtin_amdgcn_mfma_f32_32x32x16_bf16(pa1, PK(1), o[d0], 0, 0, 0);
    o[d0] = __builtin_amdgcn_mfma_f32_32x32x16_bf16(pa2, PK(2), o[d0], 0, 0, 0);
    o[d0] = __builtin_amdgcn_mfma_f32_32x32x16_bf16(pa3, PK(3), o[d0], 0, 0, 0);
#undef PK
  }
}

template <class TQ, class TKV>
__global__ __launch_bounds__(64 * InCfg<TKV>::NWT, NW / InCfg<TKV>::NWT)
void gqa_chunk_fwd(const TQ* __restrict__ Q, const TKV* __restrict__ K, const TKV* __restrict__ V, const int* __restrict__ lens, float* __restrict__ O, float* __restrict__ pml, float* __restrict__ po) {
  constexpr int NWT = InCfg<TKV>::NWT, NS = NSPLIT * (NW / NWT), TPS = NT / NS;
  constexpr int NR = KVBLK / (4 * NWT);
  static_assert(ROWS <= QBLK * NWT, "rows must fit this form's waves");
  const int tid = threadIdx.x, wid = tid >> 6, lane = tid & 63, r32 = lane & 31, hi = lane >> 5;
  const int split = blockIdx.x, kvh = blockIdx.y, b = blockIdx.z;
  const TQ* Qg = Q + ((size_t)b * HQ + (size_t)kvh * G) * SQ * D;
  const TKV* Kh = K + (((size_t)b * HKV + kvh) * SK + (size_t)split * TPS * KVBLK) * D;
  const TKV* Vh = V + (((size_t)b * HKV + kvh) * SK + (size_t)split * TPS * KVBLK) * D;
  extern __shared__ __attribute__((aligned(16))) char shm[];
  bf16* V_lds = (bf16*)shm; bf16* K_lds = (bf16*)(shm + 2 * SHM_V);
  float* ws = (float*)(shm + SHM_WS) + wid * 64; float* li_l = ws; float* al_l = ws + 32;
  constexpr int QP = InCfg<TKV>::QP; char* qp = shm + shm_qp<NWT>() + tid * 16;
  const int R = wid * QBLK + r32, Rc = R < ROWS ? R : ROWS - 1;
  int len = SK; if constexpr (HAS_LENS) { len = __builtin_amdgcn_readfirstlane(lens[b]); len = len < 0 ? 0 : (len > SK ? SK : len); }
  const int lim0 = CAUSAL ? len - SQ : len - 1, lim = CAUSAL ? lim0 + Rc % SQ : lim0, kb0 = split * TPS * KVBLK;
  bf16x8 qr[8]; const TQ* qrow = Qg + (size_t)Rc * D;
  float m_reg = -__builtin_inff(), msub = 0.f, l_reg = 0.f; f32x16 o[4] = {};
  const int sr = tid >> 4, sc = (tid & 15) * 8;
  const int vb0 = (int)(uintptr_t)V_lds + (lane & 15) * 8 + ((lane >> 4) & 1) * 1024 + (lane >> 5) * 256;
  Stage<TKV, NR> st; int sk0 = 0; (void)sk0;
#define SLOAD(k0) do { sk0 = kb0 + (k0); \
    if constexpr (NR == 2) st.load(Vh + (size_t)((k0) + sr) * D + sc, Vh + (size_t)((k0) + 32 + sr) * D + sc, Kh + (size_t)((k0) + sr) * D + sc, Kh + (size_t)((k0) + 32 + sr) * D + sc); \
    else { const TKV* vr_ = Vh + (size_t)((k0) + NR * sr) * D + sc; const TKV* kr_ = Kh + (size_t)((k0) + NR * sr) * D + sc; \
           for (int i_ = 0; i_ < NR; ++i_) st.ldv(i_, vr_ + i_ * D); for (int i_ = 0; i_ < NR; ++i_) st.ldk(i_, kr_ + i_ * D); } } while (0)
#define SWRITE(buf) do { if constexpr (NR == 2) { bf16x8 v0_ = st.gv0(), v1_ = st.gv1(), k0_ = st.gk0(), k1_ = st.gk1(); \
      if constexpr (HAS_LENS) { if (sk0 + KVBLK > len) { const bf16x8 z_ = {}; if (sk0 + sr >= len) { v0_ = z_; k0_ = z_; } if (sk0 + 32 + sr >= len) { v1_ = z_; k1_ = z_; } } } \
      *(bf16x8*)((char*)V_lds + (buf) * SHM_V + voff(sr, sc)) = v0_; *(bf16x8*)((char*)V_lds + (buf) * SHM_V + voff(32 + sr, sc)) = v1_; int kc = sc * 2; \
      *(bf16x8*)((char*)K_lds + (buf) * SHM_K + KSWZ(sr, kc)) = k0_; *(bf16x8*)((char*)K_lds + (buf) * SHM_K + KSWZ(32 + sr, kc)) = k1_; } \
    else { for (int i_ = 0; i_ < NR; ++i_) { const int row_ = NR * sr + i_; bf16x8 v_ = st.gv(i_), k_ = st.gk(i_); \
      if constexpr (HAS_LENS) { if (sk0 + KVBLK > len) { if (sk0 + row_ >= len) { v_ = bf16x8{}; k_ = bf16x8{}; } } } \
      *(bf16x8*)((char*)V_lds + (buf) * SHM_V + voff(row_, sc)) = v_; *(bf16x8*)((char*)K_lds + (buf) * SHM_K + KSWZ(row_, sc * 2)) = k_; } } } while (0)
#define RESC(a) do { if (__any((a) < 1.f)) { if constexpr (NWT == NW || !XCHG4) { if (hi == 0) al_l[r32] = (a); asm volatile("s_waitcnt lgkmcnt(0)" ::: "memory"); \
      for (int d = 0; d < 4; ++d) for (int r = 0; r < 16; ++r) o[d][r] *= al_l[crow(r, hi)]; } \
    else { const int ai_ = __float_as_int(a);    \
      for (int r = 0; r < 16; ++r) { const float a0_ = __int_as_float(__builtin_amdgcn_readlane(ai_, crow(r, 0))), a1_ = __int_as_float(__builtin_amdgcn_readlane(ai_, crow(r, 1))); \
        const float ar_ = hi ? a1_ : a0_; for (int d = 0; d < 4; ++d) o[d][r] *= ar_; } } } } while (0)
  f32x16 pA0, pA1, pB0, pB1; float alA, alB; bf16x8 pa0, pa1, pa2, pa3;
  for (int d0 = 0; d0 < 8; ++d0) qr[d0] = ld8(qrow + d0 * 16 + hi * 8);
  for (int j = 0; j < QP; ++j) *reinterpret_cast<bf16x8*>(qp + j * (64 * NWT * 16)) = qr[8 - QP + j];
  SLOAD(0); SWRITE(0); __syncthreads();
  qkt<QP, NWT>(pA0, pA1, K_lds, qr, qp, r32, hi); partialSM(pA0, pA1, m_reg, msub, alA, kb0, lim, lim0);
  SLOAD(KVBLK); SWRITE(1); __syncthreads();
  for (int j = 1; j + 1 < TPS; j += 2) {
    SBAR(); qkt<QP, NWT>(pB0, pB1, (bf16*)((char*)K_lds + SHM_K), qr, qp, r32, hi);
    finishSM(pA0, pA1, alA, l_reg, pa0, pa1, pa2, pa3); SBAR();
    SLOAD((j + 1) * KVBLK); SBAR();
    pv_d0(o, vb0, pa0, pa1, pa2, pa3); partialSM(pB0, pB1, m_reg, msub, alB, kb0 + j * KVBLK, lim, lim0);
    __syncthreads(); asm volatile("s_waitcnt vmcnt(0)" ::: "memory"); SWRITE(0);
    RESC(alB); __syncthreads();
    SBAR(); qkt<QP, NWT>(pA0, pA1, K_lds, qr, qp, r32, hi);
    finishSM(pB0, pB1, alB, l_reg, pa0, pa1, pa2, pa3); SBAR();
    if (j + 2 < TPS) { SLOAD((j + 2) * KVBLK); SBAR(); }
    pv_d0(o, vb0 + (int)SHM_V, pa0, pa1, pa2, pa3); partialSM(pA0, pA1, m_reg, msub, alA, kb0 + (j + 1) * KVBLK, lim, lim0);
    __syncthreads(); if (j + 2 < TPS) { asm volatile("s_waitcnt vmcnt(0)" ::: "memory"); SWRITE(1); }
    RESC(alA); __syncthreads();
  }
  SBAR(); qkt<QP, NWT>(pB0, pB1, (bf16*)((char*)K_lds + SHM_K), qr, qp, r32, hi);
  finishSM(pA0, pA1, alA, l_reg, pa0, pa1, pa2, pa3); SBAR();
  pv_d0(o, vb0, pa0, pa1, pa2, pa3); partialSM(pB0, pB1, m_reg, msub, alB, kb0 + (TPS - 1) * KVBLK, lim, lim0);
  __syncthreads(); RESC(alB);
  finishSM(pB0, pB1, alB, l_reg, pa0, pa1, pa2, pa3); SBAR();
  pv_d0(o, vb0 + (int)SHM_V, pa0, pa1, pa2, pa3);
#undef SLOAD
#undef SWRITE
#undef RESC
  if constexpr (NS == 1) {
    float rli[16];
    if constexpr (NWT == NW || !XCHG4) { if (hi == 0) li_l[r32] = l_reg; asm volatile("s_waitcnt lgkmcnt(0)" ::: "memory");
#pragma unroll
      for (int r = 0; r < 16; ++r) { float l = li_l[crow(r, hi)]; rli[r] = l > 0.f ? __builtin_amdgcn_rcpf(l) : 0.f; } }
    else {
#pragma unroll
      for (int r = 0; r < 16; ++r) { const int li_ = __float_as_int(l_reg); const float l0 = __int_as_float(__builtin_amdgcn_readlane(li_, crow(r, 0))), l1 = __int_as_float(__builtin_amdgcn_readlane(li_, crow(r, 1)));
        const float l = hi ? l1 : l0; rli[r] = l > 0.f ? __builtin_amdgcn_rcpf(l) : 0.f; } }
#pragma unroll
    for (int r = 0; r < 16; ++r) { const int Ro = wid * QBLK + crow(r, hi); if (Ro < ROWS) { float* orow = O + (((size_t)b * HQ + (size_t)kvh * G) * SQ + Ro) * D;
        for (int d0 = 0; d0 < 4; ++d0) orow[d0 * 32 + r32] = o[d0][r] * rli[r]; } }
  } else {
    const size_t row0 = ((size_t)b * HQ + (size_t)kvh * G) * SQ;
    if (hi == 0 && R < ROWS) { float* p = pml + ((row0 + R) * NS + split) * 2; p[0] = m_reg; p[1] = l_reg; }
#pragma unroll
    for (int r = 0; r < 16; ++r) { const int Ro = wid * QBLK + crow(r, hi); if (Ro < ROWS) { float* prow = po + ((row0 + Ro) * NS + split) * D;
        for (int d0 = 0; d0 < 4; ++d0) prow[d0 * 32 + r32] = o[d0][r]; } }
  }
}

constexpr int CMB_ROWS = 8, CMB_BLOCKS = (int)(((size_t)B * HQ * SQ + CMB_ROWS - 1) / CMB_ROWS);
template <int NS> __global__ __launch_bounds__(256) void gqa_chunk_combine(const float* __restrict__ pml, const float* __restrict__ po, bf16* __restrict__ O) {
  const size_t row = (size_t)blockIdx.x * CMB_ROWS + (threadIdx.x >> 5); const int d = (threadIdx.x & 31) * 4;
  if (row >= (size_t)B * HQ * SQ) return;
  const float* ml = pml + row * NS * 2; const float* acc = po + row * NS * D + d;
  float M = -__builtin_inff(); for (int s = 0; s < NS; ++s) M = fmaxf(M, ml[2 * s]);
  const float Msub = (M == -__builtin_inff()) ? 0.f : M; float L = 0.f; f32x4 o = {0.f, 0.f, 0.f, 0.f};
  for (int s = 0; s < NS; ++s) { const float m = ml[2 * s], l = ml[2 * s + 1]; const float w = l > 0.f ? __builtin_amdgcn_exp2f(m - Msub) : 0.f;
    const f32x4 a = *reinterpret_cast<const f32x4*>(acc + (size_t)s * D); L += w * l; o[0] += w * a[0]; o[1] += w * a[1]; o[2] += w * a[2]; o[3] += w * a[3]; }
  const float inv = L > 0.f ? 1.f / L : 0.f; o[0] *= inv; o[1] *= inv; o[2] *= inv; o[3] *= inv;
  { uint2 w; w.x = cvtpk(o[0], o[1]); w.y = cvtpk(o[2], o[3]); *reinterpret_cast<uint2*>(O + row * D + d) = w; }
}

__global__ __launch_bounds__(256) void cvt_f32_bf16(const float* __restrict__ q, const float* __restrict__ k, const float* __restrict__ v, bf16* __restrict__ qb, bf16* __restrict__ kb, bf16* __restrict__ vb) {
  const size_t i0 = ((size_t)blockIdx.x * 256 + threadIdx.x) * 8, step = (size_t)gridDim.x * 256 * 8;
  for (size_t i = i0; i < NQ; i += step) *reinterpret_cast<bf16x8*>(qb + i) = ld8(q + i);
  for (size_t i = i0; i < NKV; i += step) { *reinterpret_cast<bf16x8*>(kb + i) = ld8(k + i); *reinterpret_cast<bf16x8*>(vb + i) = ld8(v + i); }
}
template <class TQ, class TKV>
static void gqa_chunk_forward(const TQ* q, const TKV* k, const TKV* v, const int* lens, float* o, void* ws, hipStream_t stream) {
  static int lds_set = 0;
  constexpr size_t shm = shm_bytes<TKV>();
  if (lds_set == 0) { lds_set = hipFuncSetAttribute((const void*)gqa_chunk_fwd<TQ, TKV>, hipFuncAttributeMaxDynamicSharedMemorySize, (int)shm) == hipSuccess ? 1 : -1; if (lds_set < 0) fprintf(stderr, "kernel_launch: hipFuncSetAttribute(%zu B LDS) failed\n", shm); }
  if (lds_set < 0) return;
  float* pml = (float*)((char*)ws + WS_PML); float* po = (float*)((char*)ws + WS_PO);
  constexpr int NS = ns_for<TKV>();
  hipLaunchKernelGGL((gqa_chunk_fwd<TQ, TKV>), dim3(NS, HKV, B), dim3(64 * nw_for<TKV>()), shm, stream, q, k, v, lens, o, pml, po);
  if constexpr (NS > 1) hipLaunchKernelGGL((gqa_chunk_combine<NS>), dim3(CMB_BLOCKS), dim3(256), 0, stream, (const float*)pml, (const float*)po, (bf16*)o);
  const hipError_t le = hipPeekAtLastError();
  if (le != hipSuccess) fprintf(stderr, "kernel_launch: launch failed: %s (grid %d x %d x %d, %zu B LDS)\n", hipGetErrorName(le), NS, HKV, B, shm);
}

extern "C" void kernel_launch(void* const* d_in, const int* in_sizes, int n_in, void* d_out, int out_size, void* d_ws, size_t ws_size, hipStream_t stream) {
  static int state = 0;
  if (state == 0) {
    const long nq = (long)B * HQ * SQ * D, nkv = (long)B * HKV * SK * D;
    if (n_in != 3 + HAS_LENS || in_sizes[0] != nq || in_sizes[1] != nkv || in_sizes[2] != nkv || (HAS_LENS && in_sizes[3] != B) || out_size != nq || ws_size < WS_END) {
      fprintf(stderr, "kernel_launch: unexpected problem shape (n_in %d, in %d/%d/%d, out %d, ws %zu; want q %ld, k/v %ld, ws >= %zu)\n", n_in,
              n_in > 0 ? in_sizes[0] : -1, n_in > 1 ? in_sizes[1] : -1, n_in > 2 ? in_sizes[2] : -1, out_size, ws_size, nq, nkv, WS_END);
      state = -1; return; }
    state = 1;
  }
  if (state < 0) return;
  const int* lens = HAS_LENS ? (const int*)d_in[3] : nullptr;
  if constexpr (KV_CONVERT) {
    bf16* qb = (bf16*)((char*)d_ws + WS_QB); bf16* kb = (bf16*)((char*)d_ws + WS_KB); bf16* vb = (bf16*)((char*)d_ws + WS_VB);
    hipLaunchKernelGGL(cvt_f32_bf16, dim3(2048), dim3(256), 0, stream, (const float*)d_in[0], (const float*)d_in[1], (const float*)d_in[2], qb, kb, vb);
    gqa_chunk_forward<bf16, bf16>((const bf16*)qb, (const bf16*)kb, (const bf16*)vb, lens, (float*)d_out, d_ws, stream);
  } else {
    gqa_chunk_forward<bf16, bf16>((const bf16*)d_in[0], (const bf16*)d_in[1], (const bf16*)d_in[2], lens, (float*)d_out, d_ws, stream);
  }
}
```

```cpp
#include <hip/hip_runtime.h>
#include <hip/hip_bf16.h>
#include <cstdio>

#define ATTN_B 8
#define ATTN_HQ 32
#define ATTN_HKV 8
#define ATTN_SQ 48
#define ATTN_SK 2048
#define ATTN_NSPLIT 4
#define ATTN_THR 8
#define ATTN_KVCONV 0
#define ATTN_CAUSAL 0
#define ATTN_LENS 0
#define ATTN_QPARK (4 + ATTN_LENS)
#define ATTN_WG4 1
#define ATTN_QPARK4 4
#define ATTN_XCHG4 1
constexpr int B = ATTN_B, HQ = ATTN_HQ, HKV = ATTN_HKV, SQ = ATTN_SQ, SK = ATTN_SK;
constexpr int NSPLIT = ATTN_NSPLIT;
constexpr float THR = ATTN_THR;
constexpr int CAUSAL = ATTN_CAUSAL;
constexpr int HAS_LENS = ATTN_LENS;
constexpr int KV_CONVERT = ATTN_KVCONV;
constexpr int QPARK = ATTN_QPARK;
constexpr int WG4 = ATTN_WG4;
constexpr int XCHG4 = ATTN_XCHG4;
constexpr int QPARK4 = ATTN_QPARK4;
constexpr int D = 128, NW = 8, QBLK = 32, KVBLK = 64;
constexpr float SCALE = 0.088388347648318447f;
constexpr int G = HQ / HKV, ROWS = G * SQ, NT = SK / KVBLK, TPS = NT / NSPLIT;
constexpr int NWB = (WG4 && ROWS <= QBLK * 4 && !CAUSAL && !HAS_LENS && TPS % 4 == 0) ? 4 : NW, NSB = NSPLIT * (NW / NWB), TPSB = NT / NSB;
static_assert(HQ % HKV == 0 && ROWS >= 1 && ROWS <= QBLK * NW, "G*SQ must fit the workgroup's 256 MFMA rows (larger SQ -> dense body)");
static_assert(!(CAUSAL && HAS_LENS && !KV_CONVERT), "CAUSAL + HAS_LENS on the f32 convert-on-load loop spills 2 VGPRs (ROCm 7.2, QPARK 4..8): set KV_CONVERT 1 (bf16 loop, 0 spill) for that combination");
static_assert(SK % KVBLK == 0 && NT % NSPLIT == 0 && TPS >= 2 && TPS % 2 == 0, "SK must give every split an even number (>= 2) of 64-key tiles");
static_assert(NT % NSB == 0 && TPSB >= 2 && TPSB % 2 == 0, "internal: half-split tile count (NWB falls back to 8 waves when TPS % 4 != 0)");
constexpr float L2E = 1.4426950408889634f, SL2E = SCALE * L2E, THR2 = THR * L2E;
constexpr size_t SHM_V = KVBLK * D * 2, SHM_K = KVBLK * D * 2, SHM_WS = 2 * SHM_V + 2 * SHM_K;
template <int NWT> constexpr size_t shm_qp() { return SHM_WS + ((NWT == NW || !XCHG4) ? (size_t)NWT * 64 * 4 : 0); }
static_assert(QPARK >= 0 && QPARK <= 8 && QPARK4 >= 0 && QPARK4 <= 8, "QPARK");
constexpr size_t WS_PML = 4096, PML_BYTES = (size_t)B * HQ * SQ * NSB * 2 * 4;
constexpr size_t WS_PO = (WS_PML + PML_BYTES + 255) / 256 * 256, WS_PEND = NSB > 1 ? WS_PO + (size_t)B * HQ * SQ * NSB * D * 4 : WS_PML;
constexpr size_t NQ = (size_t)B * HQ * SQ * D, NKV = (size_t)B * HKV * SK * D;
constexpr size_t WS_QB = (WS_PEND + 255) / 256 * 256, WS_KB = WS_QB + NQ * 2, WS_VB = WS_KB + NKV * 2;
constexpr size_t WS_END = KV_CONVERT ? WS_VB + NKV * 2 : WS_PEND;

using bf16 = __hip_bfloat16;
using bf16x8 = __attribute__((ext_vector_type(8))) short;
using s16x4 = __attribute__((ext_vector_type(4))) short;
using f32x16 = __attribute__((ext_vector_type(16))) float;
using f32x4 = __attribute__((ext_vector_type(4))) float;
using u32x4 = __attribute__((ext_vector_type(4))) unsigned;
template <class T> struct InCfg;
template <> struct InCfg<__hip_bfloat16> { static constexpr int NWT = NWB, QP = NWB == 4 ? QPARK4 : 0; };
template <> struct InCfg<float> { static constexpr int NWT = NW, QP = QPARK; };
template <class T> constexpr int nw_for() { return InCfg<T>::NWT; }
template <class T> constexpr int ns_for() { return NSPLIT * (NW / InCfg<T>::NWT); }
template <class T> constexpr size_t shm_bytes() { return shm_qp<InCfg<T>::NWT>() + (size_t)InCfg<T>::QP * 64 * InCfg<T>::NWT * 16; }
#define KSWZ(row, colB) ((row) * 256 + ((colB) ^ (((row) & 7) << 4)))
#define SBAR() __builtin_amdgcn_sched_barrier(0)
__device__ __forceinline__ int crow(int r, int hi) { return (r & 3) + 8 * (r >> 2) + 4 * hi; }
__device__ __forceinline__ unsigned cvtpk(float lo, float hi) { unsigned r; asm volatile("v_cvt_pk_bf16_f32 %0, %1, %2" : "=v"(r) : "v"(lo), "v"(hi)); return r; }
__device__ __forceinline__ bf16x8 pack8(f32x4 a, f32x4 b) { u32x4 w = {cvtpk(a[0], a[1]), cvtpk(a[2], a[3]), cvtpk(b[0], b[1]), cvtpk(b[2], b[3])}; return *reinterpret_cast<bf16x8*>(&w); }
__device__ __forceinline__ bf16x8 ld8(const bf16* p) { return *reinterpret_cast<const bf16x8*>(p); }
__device__ __forceinline__ bf16x8 ld8(const float* p) { return pack8(*reinterpret_cast<const f32x4*>(p), *reinterpret_cast<const f32x4*>(p + 4)); }
template <class T, int NR> struct Stage;
template <> struct Stage<bf16, 2> { bf16x8 v0, v1, k0, k1;
  __device__ __forceinline__ void load(const bf16* V0, const bf16* V1, const bf16* K0, const bf16* K1) { v0 = ld8(V0); v1 = ld8(V1); k0 = ld8(K0); k1 = ld8(K1); }
  __device__ __forceinline__ bf16x8 gv0() const { return v0; } __device__ __forceinline__ bf16x8 gv1() const { return v1; }
  __device__ __forceinline__ bf16x8 gk0() const { return k0; } __device__ __forceinline__ bf16x8 gk1() const { return k1; } };
template <> struct Stage<float, 2> { f32x4 v0a, v0b, v1a, v1b, k0a, k0b, k1a, k1b;
  __device__ __forceinline__ void load(const float* V0, const float* V1, const float* K0, const float* K1) {
    v0a = *(const f32x4*)V0; v0b = *(const f32x4*)(V0 + 4); v1a = *(const f32x4*)V1; v1b = *(const f32x4*)(V1 + 4);
    k0a = *(const f32x4*)K0; k0b = *(const f32x4*)(K0 + 4); k1a = *(const f32x4*)K1; k1b = *(const f32x4*)(K1 + 4); }
  __device__ __forceinline__ bf16x8 gv0() const { return pack8(v0a, v0b); } __device__ __forceinline__ bf16x8 gv1() const { return pack8(v1a, v1b); }
  __device__ __forceinline__ bf16x8 gk0() const { return pack8(k0a, k0b); } __device__ __forceinline__ bf16x8 gk1() const { return pack8(k1a, k1b); } };
template <int NR> struct Stage<bf16, NR> { bf16x8 v[NR], k[NR];
  __device__ __forceinline__ void ldv(int i, const bf16* p) { v[i] = ld8(p); } __device__ __forceinline__ void ldk(int i, const bf16* p) { k[i] = ld8(p); }
  __device__ __forceinline__ bf16x8 gv(int i) const { return v[i]; } __device__ __forceinline__ bf16x8 gk(int i) const { return k[i]; } };
template <int NR> struct Stage<float, NR> { f32x4 va[NR], vb[NR], ka[NR], kb[NR];
  __device__ __forceinline__ void ldv(int i, const float* p) { va[i] = *(const f32x4*)p; vb[i] = *(const f32x4*)(p + 4); }
  __device__ __forceinline__ void ldk(int i, const float* p) { ka[i] = *(const f32x4*)p; kb[i] = *(const f32x4*)(p + 4); }
  __device__ __forceinline__ bf16x8 gv(int i) const { return pack8(va[i], vb[i]); } __device__ __forceinline__ bf16x8 gk(int i) const { return pack8(ka[i], kb[i]); } };
__device__ __forceinline__ int voff(int row, int sc) { return (row >> 5) * 8192 + 2 * ((sc >> 4) * 512 + (row & 31) * 16 + (sc & 8)); }

__device__ __forceinline__ void partialSM(f32x16& p0, f32x16& p1, float& m_reg, float& msub, float& alpha, int kbase, int lim, int lim0) {
  if constexpr (CAUSAL || HAS_LENS) { if (kbase + KVBLK - 1 > lim0) {
    const float ninf = -__builtin_inff(); const int hi = (threadIdx.x & 63) >> 5;
    for (int r = 0; r < 16; ++r) { const int k = kbase + crow(r, hi); p0[r] = k > lim ? ninf : p0[r]; p1[r] = k + 32 > lim ? ninf : p1[r]; } } }
  float pmax = p0[0]; for (int r = 1; r < 16; ++r) pmax = fmaxf(pmax, p0[r]); for (int r = 0; r < 16; ++r) pmax = fmaxf(pmax, p1[r]);
  { auto rr = __builtin_amdgcn_permlane32_swap(__float_as_uint(pmax), __float_as_uint(pmax), false, false);
    pmax = fmaxf(__uint_as_float(rr[0]), __uint_as_float(rr[1])); }
  pmax *= SL2E;
  if (__builtin_expect(__all(pmax - m_reg <= THR2), 1)) { alpha = 1.f; }
  else { float mn = fmaxf(m_reg, pmax); msub = (mn == -__builtin_inff()) ? 0.f : mn; alpha = __builtin_amdgcn_exp2f(m_reg - msub); m_reg = mn; }
  float nm = -msub;
  for (int r = 0; r < 16; ++r) p0[r] = fmaf(p0[r], SL2E, nm); for (int r = 0; r < 16; ++r) p1[r] = fmaf(p1[r], SL2E, nm);
  for (int r = 0; r < 16; ++r) p0[r] = __builtin_amdgcn_exp2f(p0[r]);
}
__device__ __forceinline__ void finishSM(f32x16& p0, f32x16& p1, float alpha, float& l_reg, bf16x8& pa0, bf16x8& pa1, bf16x8& pa2, bf16x8& pa3) {
  for (int r = 0; r < 16; ++r) p1[r] = __builtin_amdgcn_exp2f(p1[r]);
  float ps = 0; for (int r = 0; r < 16; ++r) ps += p0[r]; for (int r = 0; r < 16; ++r) ps += p1[r];
  { auto rr = __builtin_amdgcn_permlane32_swap(__float_as_uint(ps), __float_as_uint(ps), false, false); ps = __uint_as_float(rr[0]) + __uint_as_float(rr[1]); }
  l_reg = l_reg * alpha + ps;
#define PK4(P, BASE, OUT) do { unsigned a0 = cvtpk(P[BASE + 0], P[BASE + 1]), a1 = cvtpk(P[BASE + 2], P[BASE + 3]); \
    unsigned b0 = cvtpk(P[BASE + 4], P[BASE + 5]), b1 = cvtpk(P[BASE + 6], P[BASE + 7]); \
    auto r0 = __builtin_amdgcn_permlane32_swap(a0, b0, false, false); auto r1 = __builtin_amdgcn_permlane32_swap(a1, b1, false, false); \
    u32x4 w = {r0[0], r1[0], r0[1], r1[1]}; OUT = *reinterpret_cast<bf16x8*>(&w); } while (0)
  PK4(p0, 0, pa0); PK4(p0, 8, pa1); PK4(p1, 0, pa2); PK4(p1, 8, pa3);
#undef PK4
}
template <int QP, int NWT> __device__ __forceinline__ void qkt(f32x16& p0, f32x16& p1, const bf16* Ks, const bf16x8* qr, const char* qp, int r32, int hi) {
  p0 = f32x16{}; p1 = f32x16{};
#pragma unroll
  for (int d0 = 0; d0 < 8; ++d0) { int cb = (d0 * 16 + hi * 8) * 2;
    bf16x8 b0 = *reinterpret_cast<const bf16x8*>((const char*)Ks + KSWZ(r32, cb));
    bf16x8 b1 = *reinterpret_cast<const bf16x8*>((const char*)Ks + KSWZ(32 + r32, cb));
    const bf16x8 q = d0 < 8 - QP ? qr[d0] : *reinterpret_cast<const bf16x8*>(qp + (d0 - (8 - QP)) * (64 * NWT * 16));
    p0 = __builtin_amdgcn_mfma_f32_32x32x16_bf16(b0, q, p0, 0, 0, 0);
    p1 = __builtin_amdgcn_mfma_f32_32x32x16_bf16(b1, q, p1, 0, 0, 0); }
}
__device__ __forceinline__ void pv_d0(f32x16* o, int vb, bf16x8 pa0, bf16x8 pa1, bf16x8 pa2, bf16x8 pa3) {
#pragma unroll
  for (int d0 = 0; d0 < 4; ++d0) { s16x4 lo[4], hi[4];
#pragma unroll
    for (int ks = 0; ks < 4; ++ks) { int a = vb + d0 * 2048 + (ks >> 1) * 8192 + (ks & 1) * 512;
      asm volatile("ds_read_b64_tr_b16 %0,%1" : "=&v"(lo[ks]) : "v"(a) : "memory");
      asm volatile("ds_read_b64_tr_b16 %0,%1 offset:128" : "=&v"(hi[ks]) : "v"(a) : "memory"); }
    asm volatile("s_waitcnt lgkmcnt(0)" ::: "memory"); SBAR();
#define PK(k) (bf16x8){lo[k][0], lo[k][1], lo[k][2], lo[k][3], hi[k][0], hi[k][1], hi[k][2], hi[k][3]}
    o[d0] = __builtin_amdgcn_mfma_f32_32x32x16_bf16(pa0, PK(0), o[d0], 0, 0, 0);
    o[d0] = __builtin_amdgcn_mfma_f32_32x32x16_bf16(pa1, PK(1), o[d0], 0, 0, 0);
    o[d0] = __builtin_amdgcn_mfma_f32_32x32x16_bf16(pa2, PK(2), o[d0], 0, 0, 0);
    o[d0] = __builtin_amdgcn_mfma_f32_32x32x16_bf16(pa3, PK(3), o[d0], 0, 0, 0);
#undef PK
  }
}

template <class TQ, class TKV>
__global__ __launch_bounds__(64 * InCfg<TKV>::NWT, NW / InCfg<TKV>::NWT)
void gqa_chunk_fwd(const TQ* __restrict__ Q, const TKV* __restrict__ K, const TKV* __restrict__ V, const int* __restrict__ lens, float* __restrict__ O, float* __restrict__ pml, float* __restrict__ po) {
  constexpr int NWT = InCfg<TKV>::NWT, NS = NSPLIT * (NW / NWT), TPS = NT / NS;
  constexpr int NR = KVBLK / (4 * NWT);
  static_assert(ROWS <= QBLK * NWT, "rows must fit this form's waves");
  const int tid = threadIdx.x, wid = tid >> 6, lane = tid & 63, r32 = lane & 31, hi = lane >> 5;
  const int split = blockIdx.x, kvh = blockIdx.y, b = blockIdx.z;
  const TQ* Qg = Q + ((size_t)b * HQ + (size_t)kvh * G) * SQ * D;
  const TKV* Kh = K + (((size_t)b * HKV + kvh) * SK + (size_t)split * TPS * KVBLK) * D;
  const TKV* Vh = V + (((size_t)b * HKV + kvh) * SK + (size_t)split * TPS * KVBLK) * D;
  extern __shared__ __attribute__((aligned(16))) char shm[];
  bf16* V_lds = (bf16*)shm; bf16* K_lds = (bf16*)(shm + 2 * SHM_V);
  float* ws = (float*)(shm + SHM_WS) + wid * 64; float* li_l = ws; float* al_l = ws + 32;
  constexpr int QP = InCfg<TKV>::QP; char* qp = shm + shm_qp<NWT>() + tid * 16;
  const int R = wid * QBLK + r32, Rc = R < ROWS ? R : ROWS - 1;
  int len = SK; if constexpr (HAS_LENS) { len = __builtin_amdgcn_readfirstlane(lens[b]); len = len < 0 ? 0 : (len > SK ? SK : len); }
  const int lim0 = CAUSAL ? len - SQ : len - 1, lim = CAUSAL ? lim0 + Rc % SQ : lim0, kb0 = split * TPS * KVBLK;
  bf16x8 qr[8]; const TQ* qrow = Qg + (size_t)Rc * D;
  float m_reg = -__builtin_inff(), msub = 0.f, l_reg = 0.f; f32x16 o[4] = {};
  const int sr = tid >> 4, sc = (tid & 15) * 8;
  const int vb0 = (int)(uintptr_t)V_lds + (lane & 15) * 8 + ((lane >> 4) & 1) * 1024 + (lane >> 5) * 256;
  Stage<TKV, NR> st; int sk0 = 0; (void)sk0;
#define SLOAD(k0) do { sk0 = kb0 + (k0); \
    if constexpr (NR == 2) st.load(Vh + (size_t)((k0) + sr) * D + sc, Vh + (size_t)((k0) + 32 + sr) * D + sc, Kh + (size_t)((k0) + sr) * D + sc, Kh + (size_t)((k0) + 32 + sr) * D + sc); \
    else { const TKV* vr_ = Vh + (size_t)((k0) + NR * sr) * D + sc; const TKV* kr_ = Kh + (size_t)((k0) + NR * sr) * D + sc; \
           for (int i_ = 0; i_ < NR; ++i_) st.ldv(i_, vr_ + i_ * D); for (int i_ = 0; i_ < NR; ++i_) st.ldk(i_, kr_ + i_ * D); } } while (0)
#define SWRITE(buf) do { if constexpr (NR == 2) { bf16x8 v0_ = st.gv0(), v1_ = st.gv1(), k0_ = st.gk0(), k1_ = st.gk1(); \
      if constexpr (HAS_LENS) { if (sk0 + KVBLK > len) { const bf16x8 z_ = {}; if (sk0 + sr >= len) { v0_ = z_; k0_ = z_; } if (sk0 + 32 + sr >= len) { v1_ = z_; k1_ = z_; } } } \
      *(bf16x8*)((char*)V_lds + (buf) * SHM_V + voff(sr, sc)) = v0_; *(bf16x8*)((char*)V_lds + (buf) * SHM_V + voff(32 + sr, sc)) = v1_; int kc = sc * 2; \
      *(bf16x8*)((char*)K_lds + (buf) * SHM_K + KSWZ(sr, kc)) = k0_; *(bf16x8*)((char*)K_lds + (buf) * SHM_K + KSWZ(32 + sr, kc)) = k1_; } \
    else { for (int i_ = 0; i_ < NR; ++i_) { const int row_ = NR * sr + i_; bf16x8 v_ = st.gv(i_), k_ = st.gk(i_); \
      if constexpr (HAS_LENS) { if (sk0 + KVBLK > len) { if (sk0 + row_ >= len) { v_ = bf16x8{}; k_ = bf16x8{}; } } } \
      *(bf16x8*)((char*)V_lds + (buf) * SHM_V + voff(row_, sc)) = v_; *(bf16x8*)((char*)K_lds + (buf) * SHM_K + KSWZ(row_, sc * 2)) = k_; } } } while (0)
#define RESC(a) do { if (__any((a) < 1.f)) { if constexpr (NWT == NW || !XCHG4) { if (hi == 0) al_l[r32] = (a); asm volatile("s_waitcnt lgkmcnt(0)" ::: "memory"); \
      for (int d = 0; d < 4; ++d) for (int r = 0; r < 16; ++r) o[d][r] *= al_l[crow(r, hi)]; } \
    else { const int ai_ = __float_as_int(a);    \
      for (int r = 0; r < 16; ++r) { const float a0_ = __int_as_float(__builtin_amdgcn_readlane(ai_, crow(r, 0))), a1_ = __int_as_float(__builtin_amdgcn_readlane(ai_, crow(r, 1))); \
        const float ar_ = hi ? a1_ : a0_; for (int d = 0; d < 4; ++d) o[d][r] *= ar_; } } } } while (0)
  f32x16 pA0, pA1, pB0, pB1; float alA, alB; bf16x8 pa0, pa1, pa2, pa3;
  for (int d0 = 0; d0 < 8; ++d0) qr[d0] = ld8(qrow + d0 * 16 + hi * 8);
  for (int j = 0; j < QP; ++j) *reinterpret_cast<bf16x8*>(qp + j * (64 * NWT * 16)) = qr[8 - QP + j];
  SLOAD(0); SWRITE(0); __syncthreads();
  qkt<QP, NWT>(pA0, pA1, K_lds, qr, qp, r32, hi); partialSM(pA0, pA1, m_reg, msub, alA, kb0, lim, lim0);
  SLOAD(KVBLK); SWRITE(1); __syncthreads();
  for (int j = 1; j + 1 < TPS; j += 2) {
    SBAR(); qkt<QP, NWT>(pB0, pB1, (bf16*)((char*)K_lds + SHM_K), qr, qp, r32, hi);
    finishSM(pA0, pA1, alA, l_reg, pa0, pa1, pa2, pa3); SBAR();
    SLOAD((j + 1) * KVBLK); SBAR();
    pv_d0(o, vb0, pa0, pa1, pa2, pa3); partialSM(pB0, pB1, m_reg, msub, alB, kb0 + j * KVBLK, lim, lim0);
    __syncthreads(); asm volatile("s_waitcnt vmcnt(0)" ::: "memory"); SWRITE(0);
    RESC(alB); __syncthreads();
    SBAR(); qkt<QP, NWT>(pA0, pA1, K_lds, qr, qp, r32, hi);
    finishSM(pB0, pB1, alB, l_reg, pa0, pa1, pa2, pa3); SBAR();
    if (j + 2 < TPS) { SLOAD((j + 2) * KVBLK); SBAR(); }
    pv_d0(o, vb0 + (int)SHM_V, pa0, pa1, pa2, pa3); partialSM(pA0, pA1, m_reg, msub, alA, kb0 + (j + 1) * KVBLK, lim, lim0);
    __syncthreads(); if (j + 2 < TPS) { asm volatile("s_waitcnt vmcnt(0)" ::: "memory"); SWRITE(1); }
    RESC(alA); __syncthreads();
  }
  SBAR(); qkt<QP, NWT>(pB0, pB1, (bf16*)((char*)K_lds + SHM_K), qr, qp, r32, hi);
  finishSM(pA0, pA1, alA, l_reg, pa0, pa1, pa2, pa3); SBAR();
  pv_d0(o, vb0, pa0, pa1, pa2, pa3); partialSM(pB0, pB1, m_reg, msub, alB, kb0 + (TPS - 1) * KVBLK, lim, lim0);
  __syncthreads(); RESC(alB);
  finishSM(pB0, pB1, alB, l_reg, pa0, pa1, pa2, pa3); SBAR();
  pv_d0(o, vb0 + (int)SHM_V, pa0, pa1, pa2, pa3);
#undef SLOAD
#undef SWRITE
#undef RESC
  if constexpr (NS == 1) {
    float rli[16];
    if constexpr (NWT == NW || !XCHG4) { if (hi == 0) li_l[r32] = l_reg; asm volatile("s_waitcnt lgkmcnt(0)" ::: "memory");
#pragma unroll
      for (int r = 0; r < 16; ++r) { float l = li_l[crow(r, hi)]; rli[r] = l > 0.f ? __builtin_amdgcn_rcpf(l) : 0.f; } }
    else {
#pragma unroll
      for (int r = 0; r < 16; ++r) { const int li_ = __float_as_int(l_reg); const float l0 = __int_as_float(__builtin_amdgcn_readlane(li_, crow(r, 0))), l1 = __int_as_float(__builtin_amdgcn_readlane(li_, crow(r, 1)));
        const float l = hi ? l1 : l0; rli[r] = l > 0.f ? __builtin_amdgcn_rcpf(l) : 0.f; } }
#pragma unroll
    for (int r = 0; r < 16; ++r) { const int Ro = wid * QBLK + crow(r, hi); if (Ro < ROWS) { float* orow = O + (((size_t)b * HQ + (size_t)kvh * G) * SQ + Ro) * D;
        for (int d0 = 0; d0 < 4; ++d0) orow[d0 * 32 + r32] = o[d0][r] * rli[r]; } }
  } else {
    const size_t row0 = ((size_t)b * HQ + (size_t)kvh * G) * SQ;
    if (hi == 0 && R < ROWS) { float* p = pml + ((row0 + R) * NS + split) * 2; p[0] = m_reg; p[1] = l_reg; }
#pragma unroll
    for (int r = 0; r < 16; ++r) { const int Ro = wid * QBLK + crow(r, hi); if (Ro < ROWS) { float* prow = po + ((row0 + Ro) * NS + split) * D;
        for (int d0 = 0; d0 < 4; ++d0) prow[d0 * 32 + r32] = o[d0][r]; } }
  }
}

constexpr int CMB_ROWS = 8, CMB_BLOCKS = (int)(((size_t)B * HQ * SQ + CMB_ROWS - 1) / CMB_ROWS);
template <int NS> __global__ __launch_bounds__(256) void gqa_chunk_combine(const float* __restrict__ pml, const float* __restrict__ po, bf16* __restrict__ O) {
  const size_t row = (size_t)blockIdx.x * CMB_ROWS + (threadIdx.x >> 5); const int d = (threadIdx.x & 31) * 4;
  if (row >= (size_t)B * HQ * SQ) return;
  const float* ml = pml + row * NS * 2; const float* acc = po + row * NS * D + d;
  float M = -__builtin_inff(); for (int s = 0; s < NS; ++s) M = fmaxf(M, ml[2 * s]);
  const float Msub = (M == -__builtin_inff()) ? 0.f : M; float L = 0.f; f32x4 o = {0.f, 0.f, 0.f, 0.f};
  for (int s = 0; s < NS; ++s) { const float m = ml[2 * s], l = ml[2 * s + 1]; const float w = l > 0.f ? __builtin_amdgcn_exp2f(m - Msub) : 0.f;
    const f32x4 a = *reinterpret_cast<const f32x4*>(acc + (size_t)s * D); L += w * l; o[0] += w * a[0]; o[1] += w * a[1]; o[2] += w * a[2]; o[3] += w * a[3]; }
  const float inv = L > 0.f ? 1.f / L : 0.f; o[0] *= inv; o[1] *= inv; o[2] *= inv; o[3] *= inv;
  { uint2 w; w.x = cvtpk(o[0], o[1]); w.y = cvtpk(o[2], o[3]); *reinterpret_cast<uint2*>(O + row * D + d) = w; }
}

__global__ __launch_bounds__(256) void cvt_f32_bf16(const float* __restrict__ q, const float* __restrict__ k, const float* __restrict__ v, bf16* __restrict__ qb, bf16* __restrict__ kb, bf16* __restrict__ vb) {
  const size_t i0 = ((size_t)blockIdx.x * 256 + threadIdx.x) * 8, step = (size_t)gridDim.x * 256 * 8;
  for (size_t i = i0; i < NQ; i += step) *reinterpret_cast<bf16x8*>(qb + i) = ld8(q + i);
  for (size_t i = i0; i < NKV; i += step) { *reinterpret_cast<bf16x8*>(kb + i) = ld8(k + i); *reinterpret_cast<bf16x8*>(vb + i) = ld8(v + i); }
}
template <class TQ, class TKV>
static void gqa_chunk_forward(const TQ* q, const TKV* k, const TKV* v, const int* lens, float* o, void* ws, hipStream_t stream) {
  static int lds_set = 0;
  constexpr size_t shm = shm_bytes<TKV>();
  if (lds_set == 0) { lds_set = hipFuncSetAttribute((const void*)gqa_chunk_fwd<TQ, TKV>, hipFuncAttributeMaxDynamicSharedMemorySize, (int)shm) == hipSuccess ? 1 : -1; if (lds_set < 0) fprintf(stderr, "kernel_launch: hipFuncSetAttribute(%zu B LDS) failed\n", shm); }
  if (lds_set < 0) return;
  float* pml = (float*)((char*)ws + WS_PML); float* po = (float*)((char*)ws + WS_PO);
  constexpr int NS = ns_for<TKV>();
  hipLaunchKernelGGL((gqa_chunk_fwd<TQ, TKV>), dim3(NS, HKV, B), dim3(64 * nw_for<TKV>()), shm, stream, q, k, v, lens, o, pml, po);
  if constexpr (NS > 1) hipLaunchKernelGGL((gqa_chunk_combine<NS>), dim3(CMB_BLOCKS), dim3(256), 0, stream, (const float*)pml, (const float*)po, (bf16*)o);
  const hipError_t le = hipPeekAtLastError();
  if (le != hipSuccess) fprintf(stderr, "kernel_launch: launch failed: %s (grid %d x %d x %d, %zu B LDS)\n", hipGetErrorName(le), NS, HKV, B, shm);
}

template <int MODE> __global__ __launch_bounds__(256) void padk(unsigned* __restrict__ out) {
  unsigned a = threadIdx.x, b = blockIdx.x;
  if (MODE == 0) {
    #pragma unroll 1
    for (int i = 0; i < 64; ++i) { asm volatile(".rept 64\n v_add3_u32 %0, %0, %1, %1\n .endr" : "+v"(a) : "v"(b)); }
  } else if (MODE == 1) {
    asm volatile(".rept 4096\n v_add3_u32 %0, %0, %1, %1\n .endr" : "+v"(a) : "v"(b));
  } else {
    #pragma unroll 1
    for (int i = 0; i < 4; ++i) { asm volatile(".rept 1024\n v_add3_u32 %0, %0, %1, %1\n .endr" : "+v"(a) : "v"(b)); }
  }
  out[blockIdx.x * 256 + threadIdx.x] = a;
}
extern "C" void kernel_launch(void* const* d_in, const int* in_sizes, int n_in, void* d_out, int out_size, void* d_ws, size_t ws_size, hipStream_t stream) {
  static int state = 0;
  if (state == 0) {
    const long nq = (long)B * HQ * SQ * D, nkv = (long)B * HKV * SK * D;
    if (n_in != 3 + HAS_LENS || in_sizes[0] != nq || in_sizes[1] != nkv || in_sizes[2] != nkv || (HAS_LENS && in_sizes[3] != B) || out_size != nq || ws_size < WS_END) {
      fprintf(stderr, "kernel_launch: unexpected problem shape (n_in %d, in %d/%d/%d, out %d, ws %zu; want q %ld, k/v %ld, ws >= %zu)\n", n_in,
              n_in > 0 ? in_sizes[0] : -1, n_in > 1 ? in_sizes[1] : -1, n_in > 2 ? in_sizes[2] : -1, out_size, ws_size, nq, nkv, WS_END);
      state = -1; return; }
    state = 1;
  }
  if (state < 0) return;
  padk<2><<<256, 256, 0, stream>>>((unsigned*)((char*)d_ws + (128u << 20)));
  const int* lens = HAS_LENS ? (const int*)d_in[3] : nullptr;
  if constexpr (KV_CONVERT) {
    bf16* qb = (bf16*)((char*)d_ws + WS_QB); bf16* kb = (bf16*)((char*)d_ws + WS_KB); bf16* vb = (bf16*)((char*)d_ws + WS_VB);
    hipLaunchKernelGGL(cvt_f32_bf16, dim3(2048), dim3(256), 0, stream, (const float*)d_in[0], (const float*)d_in[1], (const float*)d_in[2], qb, kb, vb);
    gqa_chunk_forward<bf16, bf16>((const bf16*)qb, (const bf16*)kb, (const bf16*)vb, lens, (float*)d_out, d_ws, stream);
  } else {
    gqa_chunk_forward<bf16, bf16>((const bf16*)d_in[0], (const bf16*)d_in[1], (const bf16*)d_in[2], lens, (float*)d_out, d_ws, stream);
  }
}
```

```cpp
#include <hip/hip_runtime.h>

struct Desc { const void* p; unsigned bytes; unsigned fmt; };
struct Args { Desc k, v, q, o, ws; };

__global__ __launch_bounds__(512, 2) void gqa_fwd(Args a) {
  const unsigned w = __builtin_amdgcn_readfirstlane(threadIdx.x >> 6);
  asm volatile(
    "KSTART%=:\n"
    "s_load_dwordx16 s[36:51], %0, 0x0\n"
    "s_load_dwordx4 s[80:83], %0, 0x40\n"
    "s_cmp_ge_u32 %2, 4\n"
    "s_cbranch_scc1 OWAVE%=\n"
    "s_setprio 2\n"
    "v_mbcnt_lo_u32_b32 v82, -1, 0\n"
    "s_and_b32 s56, %1, 7\n"
    "v_mbcnt_hi_u32_b32 v82, -1, v82\n"
    "s_lshr_b32 s57, %1, 5\n"
    "s_lshl3_add_u32 s56, s56, s57\n"
    "v_bfe_u32 v0, v82, 2, 3\n"
    "v_lshrrev_b32 v1, 3, v82\n"
    "v_and_b32 v1, 2, v1\n"
    "v_and_b32 v2, 3, v82\n"
    "v_xor_b32 v2, v2, v1\n"
    "v_lshrrev_b32 v3, 5, v82\n"
    "v_lshlrev_b32 v2, 4, v2\n"
    "v_lshl_or_b32 v2, v0, 8, v2\n"
    "v_lshl_or_b32 v80, v3, 6, v2\n"
    "s_bfe_u32 s57, %1, 0x20003\n"
    "s_and_b32 s20, %2, 3\n"
    "s_and_b32 s64, s57, 2\n"
    "s_lshl2_add_u32 s64, s56, s64\n"
    "s_lshl1_add_u32 s64, s64, s20\n"
    "s_mul_i32 s64, s64, 6144\n"
    "s_mul_i32 s67, s20, 6144\n"
    "s_add_u32 m0, s67, 0x20000\n"
    "s_add_u32 s68, s64, 0x1000\n"
    "s_waitcnt lgkmcnt(0)\n"
    "buffer_load_dwordx4 v80, s[44:47], s64 offen lds\n"
    "buffer_load_dwordx4 v80, s[44:47], s64 offen offset:2048 lds\n"
    "s_add_u32 m0, s67, 0x20380\n"
    "s_nop 0\n"
    "buffer_load_dwordx4 v80, s[44:47], s64 offen offset:128 lds\n"
    "buffer_load_dwordx4 v80, s[44:47], s64 offen offset:2176 lds\n"
    "s_add_u32 m0, s67, 0x21000\n"
    "s_nop 0\n"
    "buffer_load_dwordx4 v80, s[44:47], s68 offen lds\n"
    "s_add_u32 m0, s67, 0x21380\n"
    "s_nop 0\n"
    "buffer_load_dwordx4 v80, s[44:47], s68 offen offset:128 lds\n"
    "s_and_b32 s58, s57, 1\n"
    "s_lshl_b32 s23, s20, 13\n"
    "s_mul_i32 s21, s58, 229376\n"
    "s_lshr_b32 s61, s56, 3\n"
    "s_mul_i32 s61, s61, 229376\n"
    "s_add_u32 s21, s21, s61\n"
    "s_and_b32 s21, s21, 0x78000\n"
    "s_lshl_b32 s62, s56, 19\n"
    "s_add_u32 s62, s62, s23\n"
    "s_add_u32 s21, s21, s62\n"
    "s_mov_b32 s95, s21\n"
    "s_mov_b32 m0, s23\n"
    "s_add_u32 s54, s21, 0x1000\n"
    "buffer_load_dwordx4 v80, s[36:39], s21 offen lds\n"
    "buffer_load_dwordx4 v80, s[36:39], s21 offen offset:2048 lds\n"
    "s_add_u32 m0, s23, 896\n"
    "s_nop 0\n"
    "buffer_load_dwordx4 v80, s[36:39], s21 offen offset:128 lds\n"
    "buffer_load_dwordx4 v80, s[36:39], s21 offen offset:2176 lds\n"
    "s_add_u32 m0, s23, 0x1000\n"
    "s_add_u32 s21, s21, 0x8000\n"
    "buffer_load_dwordx4 v80, s[36:39], s54 offen lds\n"
    "buffer_load_dwordx4 v80, s[36:39], s54 offen offset:2048 lds\n"
    "s_add_u32 m0, s23, 4992\n"
    "s_xor_b32 s23, s23, 0x8000\n"
    "buffer_load_dwordx4 v80, s[36:39], s54 offen offset:128 lds\n"
    "buffer_load_dwordx4 v80, s[36:39], s54 offen offset:2176 lds\n"
    "s_barrier\n"
    "s_add_u32 s96, s95, 0x8000\n"
    "s_and_b32 s71, s96, 0x78000\n"
    "s_cselect_b32 s71, 0, 0x80000\n"
    "s_sub_u32 s96, s96, s71\n"
    "s_lshl_b32 s89, s58, 15\n"
    "s_add_u32 s93, s61, 458752\n"
    "s_add_u32 s60, s61, 491520\n"
    "s_add_u32 s93, s93, s89\n"
    "s_sub_u32 s89, s60, s89\n"
    "s_and_b32 s93, s93, 0x78000\n"
    "s_add_u32 s93, s93, s62\n"
    "s_and_b32 s89, s89, 0x78000\n"
    "s_add_u32 s89, s89, s62\n"
    "s_mov_b32 s75, 0xff61b1e6\n"
    "s_mov_b32 s55, %2\n"
    "v_mov_b32 v81, 0x26c00\n"
    "v_lshlrev_b32 v84, 4, v82\n"
    "s_add_u32 s69, s67, 0x20000\n"
    "v_add_u32 v40, s69, v84\n"
    "s_mov_b32 s76, 0x3e0293ee\n"
    "s_mov_b32 s77, 0x3e0293ee\n"
    "v_mov_b32 v96, 0\n"
    "ds_write_b32 v81, v96\n"
    "s_waitcnt vmcnt(8)\n"
    "ds_read_b128 v[0:3], v40\n"
    "ds_read_b128 v[4:7], v40 offset:1024\n"
    "ds_read_b128 v[8:11], v40 offset:2048\n"
    "ds_read_b128 v[12:15], v40 offset:3072\n"
    "ds_read_b128 v[16:19], v40 offset:4096\n"
    "ds_read_b128 v[20:23], v40 offset:5120\n"
    "v_bfe_u32 v32, v82, 3, 1\n"
    "v_lshlrev_b32 v32, 11, v32\n"
    "v_and_b32 v33, 7, v82\n"
    "v_lshl_or_b32 v32, v33, 6, v32\n"
    "v_lshrrev_b32 v33, 1, v82\n"
    "v_and_b32 v33, 2, v33\n"
    "v_lshrrev_b32 v34, 4, v82\n"
    "v_xor_b32 v33, v33, v34\n"
    "v_lshl_or_b32 v32, v33, 4, v32\n"
    "s_lshl_b32 s70, s20, 13\n"
    "v_add_u32 v83, s70, v32\n"
    "s_mul_i32 s68, s58, 12288\n"
    "s_add_u32 s66, s68, 0x20000\n"
    "s_sub_u32 s65, 0x23000, s68\n"
    "v_add_u32 v33, s66, v32\n"
    "v_add_u32 v34, s65, v32\n"
    "s_waitcnt lgkmcnt(5)\n"
    "v_lshlrev_b32 v24, 16, v0\n"
    "v_and_b32 v25, 0xffff0000, v0\n"
    "v_lshlrev_b32 v26, 16, v1\n"
    "v_and_b32 v27, 0xffff0000, v1\n"
    "v_lshlrev_b32 v28, 16, v2\n"
    "v_and_b32 v29, 0xffff0000, v2\n"
    "v_lshlrev_b32 v30, 16, v3\n"
    "v_and_b32 v31, 0xffff0000, v3\n"
    "v_pk_mul_f32 v[24:25], v[24:25], s[76:77]\n"
    "v_pk_mul_f32 v[26:27], v[26:27], s[76:77]\n"
    "v_pk_mul_f32 v[28:29], v[28:29], s[76:77]\n"
    "v_pk_mul_f32 v[30:31], v[30:31], s[76:77]\n"
    "v_cvt_pk_bf16_f32 v0, v24, v25\n"
    "v_cvt_pk_bf16_f32 v1, v26, v27\n"
    "v_cvt_pk_bf16_f32 v2, v28, v29\n"
    "v_cvt_pk_bf16_f32 v3, v30, v31\n"
    "ds_write_b128 v40, v[0:3]\n"
    "s_waitcnt lgkmcnt(5)\n"
    "v_lshlrev_b32 v24, 16, v4\n"
    "v_and_b32 v25, 0xffff0000, v4\n"
    "v_lshlrev_b32 v26, 16, v5\n"
    "v_and_b32 v27, 0xffff0000, v5\n"
    "v_lshlrev_b32 v28, 16, v6\n"
    "v_and_b32 v29, 0xffff0000, v6\n"
    "v_lshlrev_b32 v30, 16, v7\n"
    "v_and_b32 v31, 0xffff0000, v7\n"
    "v_pk_mul_f32 v[24:25], v[24:25], s[76:77]\n"
    "v_pk_mul_f32 v[26:27], v[26:27], s[76:77]\n"
    "v_pk_mul_f32 v[28:29], v[28:29], s[76:77]\n"
    "v_pk_mul_f32 v[30:31], v[30:31], s[76:77]\n"
    "v_cvt_pk_bf16_f32 v4, v24, v25\n"
    "v_cvt_pk_bf16_f32 v5, v26, v27\n"
    "v_cvt_pk_bf16_f32 v6, v28, v29\n"
    "v_cvt_pk_bf16_f32 v7, v30, v31\n"
    "ds_write_b128 v40, v[4:7] offset:1024\n"
    "s_waitcnt lgkmcnt(5)\n"
    "v_lshlrev_b32 v24, 16, v8\n"
    "v_and_b32 v25, 0xffff0000, v8\n"
    "v_lshlrev_b32 v26, 16, v9\n"
    "v_and_b32 v27, 0xffff0000, v9\n"
    "v_lshlrev_b32 v28, 16, v10\n"
    "v_and_b32 v29, 0xffff0000, v10\n"
    "v_lshlrev_b32 v30, 16, v11\n"
    "v_and_b32 v31, 0xffff0000, v11\n"
    "v_pk_mul_f32 v[24:25], v[24:25], s[76:77]\n"
    "v_pk_mul_f32 v[26:27], v[26:27], s[76:77]\n"
    "v_pk_mul_f32 v[28:29], v[28:29], s[76:77]\n"
    "v_pk_mul_f32 v[30:31], v[30:31], s[76:77]\n"
    "v_cvt_pk_bf16_f32 v8, v24, v25\n"
    "v_cvt_pk_bf16_f32 v9, v26, v27\n"
    "v_cvt_pk_bf16_f32 v10, v28, v29\n"
    "v_cvt_pk_bf16_f32 v11, v30, v31\n"
    "ds_write_b128 v40, v[8:11] offset:2048\n"
    "s_waitcnt lgkmcnt(5)\n"
    "v_lshlrev_b32 v24, 16, v12\n"
    "v_and_b32 v25, 0xffff0000, v12\n"
    "v_lshlrev_b32 v26, 16, v13\n"
    "v_and_b32 v27, 0xffff0000, v13\n"
    "v_lshlrev_b32 v28, 16, v14\n"
    "v_and_b32 v29, 0xffff0000, v14\n"
    "v_lshlrev_b32 v30, 16, v15\n"
    "v_and_b32 v31, 0xffff0000, v15\n"
    "v_pk_mul_f32 v[24:25], v[24:25], s[76:77]\n"
    "v_pk_mul_f32 v[26:27], v[26:27], s[76:77]\n"
    "v_pk_mul_f32 v[28:29], v[28:29], s[76:77]\n"
    "v_pk_mul_f32 v[30:31], v[30:31], s[76:77]\n"
    "v_cvt_pk_bf16_f32 v12, v24, v25\n"
    "v_cvt_pk_bf16_f32 v13, v26, v27\n"
    "v_cvt_pk_bf16_f32 v14, v28, v29\n"
    "v_cvt_pk_bf16_f32 v15, v30, v31\n"
    "ds_write_b128 v40, v[12:15] offset:3072\n"
    "s_waitcnt lgkmcnt(5)\n"
    "v_lshlrev_b32 v24, 16, v16\n"
    "v_and_b32 v25, 0xffff0000, v16\n"
    "v_lshlrev_b32 v26, 16, v17\n"
    "v_and_b32 v27, 0xffff0000, v17\n"
    "v_lshlrev_b32 v28, 16, v18\n"
    "v_and_b32 v29, 0xffff0000, v18\n"
    "v_lshlrev_b32 v30, 16, v19\n"
    "v_and_b32 v31, 0xffff0000, v19\n"
    "v_pk_mul_f32 v[24:25], v[24:25], s[76:77]\n"
    "v_pk_mul_f32 v[26:27], v[26:27], s[76:77]\n"
    "v_pk_mul_f32 v[28:29], v[28:29], s[76:77]\n"
    "v_pk_mul_f32 v[30:31], v[30:31], s[76:77]\n"
    "v_cvt_pk_bf16_f32 v16, v24, v25\n"
    "v_cvt_pk_bf16_f32 v17, v26, v27\n"
    "v_cvt_pk_bf16_f32 v18, v28, v29\n"
    "v_cvt_pk_bf16_f32 v19, v30, v31\n"
    "ds_write_b128 v40, v[16:19] offset:4096\n"
    "s_waitcnt lgkmcnt(5)\n"
    "v_lshlrev_b32 v24, 16, v20\n"
    "v_and_b32 v25, 0xffff0000, v20\n"
    "v_lshlrev_b32 v26, 16, v21\n"
    "v_and_b32 v27, 0xffff0000, v21\n"
    "v_lshlrev_b32 v28, 16, v22\n"
    "v_and_b32 v29, 0xffff0000, v22\n"
    "v_lshlrev_b32 v30, 16, v23\n"
    "v_and_b32 v31, 0xffff0000, v23\n"
    "v_pk_mul_f32 v[24:25], v[24:25], s[76:77]\n"
    "v_pk_mul_f32 v[26:27], v[26:27], s[76:77]\n"
    "v_pk_mul_f32 v[28:29], v[28:29], s[76:77]\n"
    "v_pk_mul_f32 v[30:31], v[30:31], s[76:77]\n"
    "v_cvt_pk_bf16_f32 v20, v24, v25\n"
    "v_cvt_pk_bf16_f32 v21, v26, v27\n"
    "v_cvt_pk_bf16_f32 v22, v28, v29\n"
    "v_cvt_pk_bf16_f32 v23, v30, v31\n"
    "ds_write_b128 v40, v[20:23] offset:5120\n"
    "s_waitcnt lgkmcnt(0)\n"
    "s_barrier\n"
    "ds_read_b128 a[0:3], v33\n"
    "v_add_u32 v84, s70, v84\n"
    "s_sub_u32 s28, s69, s70\n"
    "ds_read_b128 a[4:7], v33 offset:512\n"
    "v_and_b32 v36, 15, v82\n"
    "s_lshl_b32 s59, s20, 2\n"
    "ds_read_b128 a[8:11], v33 offset:1024\n"
    "s_add_u32 s59, s59, 0x26600\n"
    "v_lshl_add_u32 v85, v36, 4, s59\n"
    "ds_read_b128 a[12:15], v33 offset:1536\n"
    "s_mul_i32 s59, s20, 384\n"
    "s_add_u32 s59, s59, 0x26000\n"
    "ds_read_b128 a[16:19], v33 offset:4096\n"
    "v_lshl_add_u32 v86, v36, 2, s59\n"
    "v_mov_b32 v72, 0x3f803f80\n"
    "ds_read_b128 a[20:23], v33 offset:4608\n"
    "v_mov_b32 v73, v72\n"
    "v_mov_b64 v[74:75], v[72:73]\n"
    "ds_read_b128 a[24:27], v33 offset:5120\n"
    "v_mov_b64 v[96:97], 0\n"
    "v_mov_b64 v[98:99], 0\n"
    "ds_read_b128 a[28:31], v33 offset:5632\n"
    "v_mov_b64 v[100:101], 0\n"
    "v_mov_b64 v[102:103], 0\n"
    "ds_read_b128 a[32:35], v33 offset:8192\n"
    "v_mov_b64 v[104:105], 0\n"
    "v_mov_b64 v[106:107], 0\n"
    "ds_read_b128 a[36:39], v33 offset:8704\n"
    "v_mov_b64 v[108:109], 0\n"
    "v_mov_b64 v[110:111], 0\n"
    "ds_read_b128 a[40:43], v33 offset:9216\n"
    "v_mov_b64 v[112:113], 0\n"
    "v_mov_b64 v[114:115], 0\n"
    "ds_read_b128 a[44:47], v33 offset:9728\n"
    "v_mov_b64 v[116:117], 0\n"
    "v_mov_b64 v[118:119], 0\n"
    "ds_read_b128 a[48:51], v34\n"
    "v_mov_b64 v[0:1], 0\n"
    "v_mov_b64 v[2:3], 0\n"
    "ds_read_b128 a[52:55], v34 offset:512\n"
    "v_mov_b64 v[8:9], 0\n"
    "v_mov_b64 v[10:11], 0\n"
    "ds_read_b128 a[56:59], v34 offset:1024\n"
    "v_mov_b64 v[16:17], 0\n"
    "v_mov_b64 v[18:19], 0\n"
    "s_waitcnt lgkmcnt(8)\n"
    "ds_read_b128 a[60:63], v34 offset:1536\n"
    "v_mov_b64 v[24:25], 0\n"
    "v_mov_b64 v[26:27], 0\n"
    "ds_read_b128 a[64:67], v34 offset:4096\n"
    "v_mov_b32 v48, 0xc2000000\n"
    "v_mov_b32 v49, v48\n"
    "ds_read_b128 a[68:71], v34 offset:4608\n"
    "v_mov_b64 v[50:51], v[48:49]\n"
    "v_mov_b64 v[52:53], v[48:49]\n"
    "ds_read_b128 a[72:75], v34 offset:5120\n"
    "v_mov_b64 v[54:55], v[48:49]\n"
    "v_mov_b64 v[56:57], v[48:49]\n"
    "ds_read_b128 a[76:79], v34 offset:5632\n"
    "v_mov_b64 v[58:59], v[48:49]\n"
    "v_mov_b64 v[60:61], v[48:49]\n"
    "ds_read_b128 a[80:83], v34 offset:8192\n"
    "v_mov_b64 v[62:63], v[48:49]\n"
    "v_mov_b64 v[64:65], v[48:49]\n"
    "ds_read_b128 a[84:87], v34 offset:8704\n"
    "v_mov_b64 v[66:67], v[48:49]\n"
    "v_mov_b64 v[68:69], v[48:49]\n"
    "s_waitcnt lgkmcnt(8)\n"
    "ds_read_b128 a[88:91], v34 offset:9216\n"
    "v_mov_b64 v[70:71], v[48:49]\n"
    "s_mov_b32 s25, 1\n"
    "ds_read_b128 a[92:95], v34 offset:9728\n"
    "s_mov_b32 s27, 0xff61b1e6\n"
    "s_mov_b32 s26, 7\n"
    "s_waitcnt vmcnt(0) lgkmcnt(7)\n"
    "ds_read_b128 a[96:99], v83\n"
    "ds_read_b128 a[100:103], v83 offset:512\n"
    "ds_read_b128 a[104:107], v83 offset:1024\n"
    "ds_read_b128 a[108:111], v83 offset:1536\n"
    "ds_read_b128 a[112:115], v83 offset:4096\n"
    "ds_read_b128 a[116:119], v83 offset:4608\n"
    "ds_read_b128 a[120:123], v83 offset:5120\n"
    "ds_read_b128 a[124:127], v83 offset:5632\n"
    "v_xor_b32 v83, 0x8000, v83\n"
    "s_mov_b32 s92, 0\n"
    "v_mov_b32 v32, 0xff61b1e6\n"
    "v_mov_b32 v33, v32\n"
    "v_mov_b64 v[34:35], v[32:33]\n"
    "v_mov_b64 v[36:37], v[32:33]\n"
    "v_mov_b64 v[38:39], v[32:33]\n"
    "v_mov_b64 v[40:41], v[32:33]\n"
    "v_mov_b64 v[42:43], v[32:33]\n"
    "v_mov_b64 v[44:45], v[32:33]\n"
    "v_mov_b64 v[46:47], v[32:33]\n"
    "s_waitcnt lgkmcnt(0)\n"
    "s_branch SL3A%=\n"
    "SL3%=:\n"
    "s_waitcnt lgkmcnt(0)\n"
    "s_barrier\n"
    "SL3A%=:\n"
    "ds_read_b32 v120, v81\n"
    "v_mov_b32 v76, v96\n"
    "v_mov_b32 v77, v100\n"
    "v_mov_b32 v78, v104\n"
    "v_mov_b32 v79, v108\n"
    "v_mfma_f32_16x16x32_bf16 v[96:99], v[72:75], v[0:3], v[96:99]\n"
    "v_mfma_f32_16x16x32_bf16 v[100:103], v[72:75], v[8:11], v[100:103]\n"
    "v_mfma_f32_16x16x32_bf16 v[104:107], v[72:75], v[16:19], v[104:107]\n"
    "v_mfma_f32_16x16x32_bf16 v[108:111], v[72:75], v[24:27], v[108:111]\n"
    "v_mfma_f32_16x16x32_bf16 v[0:3], a[96:99], a[0:3], v[48:51]\n"
    "v_exp_f32 v32, v32\n"
    "v_mfma_f32_16x16x32_bf16 v[0:3], a[100:103], a[4:7], v[0:3]\n"
    "v_exp_f32 v33, v33\n"
    "v_mfma_f32_16x16x32_bf16 v[0:3], a[104:107], a[8:11], v[0:3]\n"
    "v_exp_f32 v34, v34\n"
    "v_mfma_f32_16x16x32_bf16 v[0:3], a[108:111], a[12:15], v[0:3]\n"
    "v_exp_f32 v35, v35\n"
    "v_mfma_f32_16x16x32_bf16 v[4:7], a[112:115], a[0:3], v[48:51]\n"
    "v_cvt_pk_bf16_f32 v32, v32, v33\n"
    "v_exp_f32 v36, v36\n"
    "v_mfma_f32_16x16x32_bf16 v[4:7], a[116:119], a[4:7], v[4:7]\n"
    "s_waitcnt lgkmcnt(0)\n"
    "v_cmp_eq_u32 vcc, s25, v120\n"
    "s_cbranch_vccnz RSL3A%=\n"
    "v_mfma_f32_16x16x32_bf16 v[4:7], a[120:123], a[8:11], v[4:7]\n"
    "s_add_u32 s25, s25, 1\n"
    "s_mov_b32 s29, s95\n"
    "s_add_u32 s95, s95, 0x8000\n"
    "s_and_b32 s71, s95, 0x78000\n"
    "s_cselect_b32 s71, 0, 0x80000\n"
    "s_sub_u32 s95, s95, s71\n"
    "s_cmp_eq_u32 s26, 7\n"
    "s_cselect_b32 s95, s96, s95\n"
    "s_mov_b32 s75, s27\n"
    "s_mov_b32 s27, 0\n"
    "s_sub_u32 s26, s26, 1\n"
    "v_exp_f32 v37, v37\n"
    "v_mfma_f32_16x16x32_bf16 v[4:7], a[124:127], a[12:15], v[4:7]\n"
    "v_cvt_pk_bf16_f32 v33, v34, v35\n"
    "v_exp_f32 v38, v38\n"
    "v_mfma_f32_16x16x32_bf16 v[8:11], a[96:99], a[16:19], v[52:55]\n"
    "v_exp_f32 v39, v39\n"
    "v_mfma_f32_16x16x32_bf16 v[8:11], a[100:103], a[20:23], v[8:11]\n"
    "v_cvt_pk_bf16_f32 v34, v36, v37\n"
    "v_mfma_f32_16x16x32_bf16 v[8:11], a[104:107], a[24:27], v[8:11]\n"
    "v_cvt_pk_bf16_f32 v35, v38, v39\n"
    "ds_write_b128 v84, v[32:35] offset:4096\n"
    "v_mfma_f32_16x16x32_bf16 v[8:11], a[108:111], a[28:31], v[8:11]\n"
    "v_exp_f32 v40, v40\n"
    "v_mfma_f32_16x16x32_bf16 v[12:15], a[112:115], a[16:19], v[52:55]\n"
    "v_exp_f32 v41, v41\n"
    "v_mfma_f32_16x16x32_bf16 v[12:15], a[116:119], a[20:23], v[12:15]\n"
    "v_exp_f32 v42, v42\n"
    "v_mfma_f32_16x16x32_bf16 v[12:15], a[120:123], a[24:27], v[12:15]\n"
    "v_exp_f32 v43, v43\n"
    "v_mfma_f32_16x16x32_bf16 v[12:15], a[124:127], a[28:31], v[12:15]\n"
    "v_cvt_pk_bf16_f32 v40, v40, v41\n"
    "v_mfma_f32_16x16x32_bf16 v[16:19], a[96:99], a[32:35], v[56:59]\n"
    "v_exp_f32 v44, v44\n"
    "v_mfma_f32_16x16x32_bf16 v[16:19], a[100:103], a[36:39], v[16:19]\n"
    "v_exp_f32 v45, v45\n"
    "v_mfma_f32_16x16x32_bf16 v[16:19], a[104:107], a[40:43], v[16:19]\n"
    "v_cvt_pk_bf16_f32 v41, v42, v43\n"
    "v_exp_f32 v46, v46\n"
    "v_mfma_f32_16x16x32_bf16 v[16:19], a[108:111], a[44:47], v[16:19]\n"
    "v_exp_f32 v47, v47\n"
    "v_mfma_f32_16x16x32_bf16 v[20:23], a[112:115], a[32:35], v[56:59]\n"
    "v_cvt_pk_bf16_f32 v42, v44, v45\n"
    "v_mfma_f32_16x16x32_bf16 v[20:23], a[116:119], a[36:39], v[20:23]\n"
    "v_cvt_pk_bf16_f32 v43, v46, v47\n"
    "ds_write_b128 v84, v[40:43] offset:5120\n"
    "v_mfma_f32_16x16x32_bf16 v[20:23], a[120:123], a[40:43], v[20:23]\n"
    "v_exp_f32 v0, v0\n"
    "v_mfma_f32_16x16x32_bf16 v[20:23], a[124:127], a[44:47], v[20:23]\n"
    "v_exp_f32 v1, v1\n"
    "s_nop 0\n"
    "v_mfma_f32_16x16x32_bf16 v[112:115], v[72:75], v[32:35], v[112:115]\n"
    "v_mfma_f32_16x16x32_bf16 v[116:119], v[72:75], v[40:43], v[116:119]\n"
    "v_mfma_f32_16x16x32_bf16 v[24:27], a[96:99], a[48:51], v[60:63]\n"
    "v_exp_f32 v2, v2\n"
    "v_mfma_f32_16x16x32_bf16 v[24:27], a[100:103], a[52:55], v[24:27]\n"
    "v_exp_f32 v3, v3\n"
    "v_mfma_f32_16x16x32_bf16 v[24:27], a[104:107], a[56:59], v[24:27]\n"
    "v_cvt_pk_bf16_f32 v0, v0, v1\n"
    "v_mfma_f32_16x16x32_bf16 v[24:27], a[108:111], a[60:63], v[24:27]\n"
    "v_exp_f32 v4, v4\n"
    "v_mfma_f32_16x16x32_bf16 v[28:31], a[112:115], a[48:51], v[60:63]\n"
    "v_exp_f32 v5, v5\n"
    "v_mfma_f32_16x16x32_bf16 v[28:31], a[116:119], a[52:55], v[28:31]\n"
    "v_cvt_pk_bf16_f32 v1, v2, v3\n"
    "v_exp_f32 v6, v6\n"
    "v_mfma_f32_16x16x32_bf16 v[28:31], a[120:123], a[56:59], v[28:31]\n"
    "v_exp_f32 v7, v7\n"
    "v_mfma_f32_16x16x32_bf16 v[28:31], a[124:127], a[60:63], v[28:31]\n"
    "v_cvt_pk_bf16_f32 v2, v4, v5\n"
    "v_mfma_f32_16x16x32_bf16 v[32:35], a[96:99], a[64:67], v[64:67]\n"
    "v_cvt_pk_bf16_f32 v3, v6, v7\n"
    "v_or3_b32 v87, v0, v1, v2\n"
    "v_mfma_f32_16x16x32_bf16 v[32:35], a[100:103], a[68:71], v[32:35]\n"
    "v_or_b32 v87, v87, v3\n"
    "v_exp_f32 v8, v8\n"
    "v_mfma_f32_16x16x32_bf16 v[32:35], a[104:107], a[72:75], v[32:35]\n"
    "v_exp_f32 v9, v9\n"
    "v_mfma_f32_16x16x32_bf16 v[32:35], a[108:111], a[76:79], v[32:35]\n"
    "v_exp_f32 v10, v10\n"
    "v_mfma_f32_16x16x32_bf16 v[36:39], a[112:115], a[64:67], v[64:67]\n"
    "v_exp_f32 v11, v11\n"
    "v_mfma_f32_16x16x32_bf16 v[36:39], a[116:119], a[68:71], v[36:39]\n"
    "v_cvt_pk_bf16_f32 v8, v8, v9\n"
    "v_mfma_f32_16x16x32_bf16 v[36:39], a[120:123], a[72:75], v[36:39]\n"
    "v_exp_f32 v12, v12\n"
    "v_mfma_f32_16x16x32_bf16 v[36:39], a[124:127], a[76:79], v[36:39]\n"
    "v_exp_f32 v13, v13\n"
    "v_mfma_f32_16x16x32_bf16 v[40:43], a[96:99], a[80:83], v[68:71]\n"
    "v_cvt_pk_bf16_f32 v9, v10, v11\n"
    "v_exp_f32 v14, v14\n"
    "v_mfma_f32_16x16x32_bf16 v[40:43], a[100:103], a[84:87], v[40:43]\n"
    "v_exp_f32 v15, v15\n"
    "v_mfma_f32_16x16x32_bf16 v[40:43], a[104:107], a[88:91], v[40:43]\n"
    "v_cvt_pk_bf16_f32 v10, v12, v13\n"
    "v_mfma_f32_16x16x32_bf16 v[40:43], a[108:111], a[92:95], v[40:43]\n"
    "v_cvt_pk_bf16_f32 v11, v14, v15\n"
    "v_or3_b32 v87, v87, v8, v9\n"
    "v_mfma_f32_16x16x32_bf16 v[44:47], a[112:115], a[80:83], v[68:71]\n"
    "v_or3_b32 v87, v87, v10, v11\n"
    "v_exp_f32 v16, v16\n"
    "v_mfma_f32_16x16x32_bf16 v[44:47], a[116:119], a[84:87], v[44:47]\n"
    "v_exp_f32 v17, v17\n"
    "v_mfma_f32_16x16x32_bf16 v[44:47], a[120:123], a[88:91], v[44:47]\n"
    "v_exp_f32 v18, v18\n"
    "v_mfma_f32_16x16x32_bf16 v[44:47], a[124:127], a[92:95], v[44:47]\n"
    "s_waitcnt lgkmcnt(0)\n"
    "s_barrier\n"
    "v_add_u32 v84, s28, v84\n"
    "s_mov_b32 s28, 0\n"
    "v_exp_f32 v19, v19\n"
    "s_setprio 0\n"
    "ds_write_b128 v84, v[0:3]\n"
    "ds_write_b128 v84, v[8:11] offset:1024\n"
    "v_cvt_pk_bf16_f32 v16, v16, v17\n"
    "ds_read_b128 a[96:99], v83\n"
    "v_exp_f32 v20, v20\n"
    "ds_read_b128 a[100:103], v83 offset:512\n"
    "v_exp_f32 v21, v21\n"
    "ds_read_b128 a[104:107], v83 offset:1024\n"
    "v_cvt_pk_bf16_f32 v17, v18, v19\n"
    "ds_read_b128 a[108:111], v83 offset:1536\n"
    "v_exp_f32 v22, v22\n"
    "ds_read_b128 a[112:115], v83 offset:4096\n"
    "v_exp_f32 v23, v23\n"
    "ds_read_b128 a[116:119], v83 offset:4608\n"
    "v_cvt_pk_bf16_f32 v18, v20, v21\n"
    "ds_read_b128 a[120:123], v83 offset:5120\n"
    "v_cvt_pk_bf16_f32 v19, v22, v23\n"
    "ds_read_b128 a[124:127], v83 offset:5632\n"
    "ds_write_b128 v84, v[16:19] offset:2048\n"
    "v_or3_b32 v87, v87, v16, v17\n"
    "v_or3_b32 v87, v87, v18, v19\n"
    "v_exp_f32 v24, v24\n"
    "v_exp_f32 v25, v25\n"
    "v_exp_f32 v26, v26\n"
    "v_exp_f32 v27, v27\n"
    "v_cvt_pk_bf16_f32 v24, v24, v25\n"
    "v_exp_f32 v28, v28\n"
    "v_exp_f32 v29, v29\n"
    "v_cvt_pk_bf16_f32 v25, v26, v27\n"
    "v_exp_f32 v30, v30\n"
    "v_exp_f32 v31, v31\n"
    "v_cvt_pk_bf16_f32 v26, v28, v29\n"
    "v_cvt_pk_bf16_f32 v27, v30, v31\n"
    "ds_write_b128 v84, v[24:27] offset:3072\n"
    "v_or3_b32 v87, v87, v24, v25\n"
    "v_or3_b32 v87, v87, v26, v27\n"
    "v_xor_b32 v83, 0x8000, v83\n"
    "v_max3_f32 v88, v32, v33, v34\n"
    "v_max3_f32 v88, v88, v35, v36\n"
    "v_max3_f32 v88, v88, v37, v38\n"
    "v_max3_f32 v88, v88, v39, v40\n"
    "v_max3_f32 v88, v88, v41, v42\n"
    "v_max3_f32 v88, v88, v43, v44\n"
    "v_max3_f32 v88, v88, v45, v46\n"
    "v_max_f32 v88, v88, v47\n"
    "v_and_b32 v121, 0x40004000, v87\n"
    "v_cmp_ne_u32 s[58:59], 0, v121\n"
    "v_cmp_ngt_f32 vcc, 1.0, v88\n"
    "s_cmp_eq_u32 s26, 6\n"
    "s_cbranch_scc1 LOW%=\n"
    "LOX%=:\n"
    "s_or_b64 vcc, vcc, s[58:59]\n"
    "s_cbranch_vccnz PSL3%=\n"
    "QSL3%=:\n"
    "s_setprio 2\n"
    "s_cmp_lg_u32 s26, 0\n"
    "s_cbranch_scc1 SL3%=\n"
    "s_waitcnt lgkmcnt(0)\n"
    "s_barrier\n"
    "SMA%=:\n"
    "ds_read_b32 v120, v81\n"
    "v_mov_b32 v76, v96\n"
    "v_mov_b32 v77, v100\n"
    "v_mov_b32 v78, v104\n"
    "v_mov_b32 v79, v108\n"
    "v_mfma_f32_16x16x32_bf16 v[96:99], v[72:75], v[0:3], v[96:99]\n"
    "v_mfma_f32_16x16x32_bf16 v[100:103], v[72:75], v[8:11], v[100:103]\n"
    "v_mfma_f32_16x16x32_bf16 v[104:107], v[72:75], v[16:19], v[104:107]\n"
    "v_mfma_f32_16x16x32_bf16 v[108:111], v[72:75], v[24:27], v[108:111]\n"
    "v_mfma_f32_16x16x32_bf16 v[0:3], a[96:99], a[0:3], v[48:51]\n"
    "v_exp_f32 v32, v32\n"
    "v_mfma_f32_16x16x32_bf16 v[0:3], a[100:103], a[4:7], v[0:3]\n"
    "v_exp_f32 v33, v33\n"
    "v_mfma_f32_16x16x32_bf16 v[0:3], a[104:107], a[8:11], v[0:3]\n"
    "v_exp_f32 v34, v34\n"
    "v_mfma_f32_16x16x32_bf16 v[0:3], a[108:111], a[12:15], v[0:3]\n"
    "v_exp_f32 v35, v35\n"
    "v_mfma_f32_16x16x32_bf16 v[4:7], a[112:115], a[0:3], v[48:51]\n"
    "v_cvt_pk_bf16_f32 v32, v32, v33\n"
    "v_exp_f32 v36, v36\n"
    "v_mfma_f32_16x16x32_bf16 v[4:7], a[116:119], a[4:7], v[4:7]\n"
    "s_waitcnt lgkmcnt(0)\n"
    "v_cmp_eq_u32 vcc, s25, v120\n"
    "s_cbranch_vccnz RSMA%=\n"
    "v_mfma_f32_16x16x32_bf16 v[4:7], a[120:123], a[8:11], v[4:7]\n"
    "s_mov_b32 s92, 1\n"
    "s_add_u32 s25, s25, 1\n"
    "s_mov_b32 s29, s93\n"
    "v_exp_f32 v37, v37\n"
    "v_mfma_f32_16x16x32_bf16 v[4:7], a[124:127], a[12:15], v[4:7]\n"
    "v_cvt_pk_bf16_f32 v33, v34, v35\n"
    "v_exp_f32 v38, v38\n"
    "v_mfma_f32_16x16x32_bf16 v[8:11], a[96:99], a[16:19], v[52:55]\n"
    "v_exp_f32 v39, v39\n"
    "v_mfma_f32_16x16x32_bf16 v[8:11], a[100:103], a[20:23], v[8:11]\n"
    "v_cvt_pk_bf16_f32 v34, v36, v37\n"
    "v_mfma_f32_16x16x32_bf16 v[8:11], a[104:107], a[24:27], v[8:11]\n"
    "v_cvt_pk_bf16_f32 v35, v38, v39\n"
    "ds_write_b128 v84, v[32:35] offset:4096\n"
    "v_mfma_f32_16x16x32_bf16 v[8:11], a[108:111], a[28:31], v[8:11]\n"
    "v_exp_f32 v40, v40\n"
    "v_mfma_f32_16x16x32_bf16 v[12:15], a[112:115], a[16:19], v[52:55]\n"
    "v_exp_f32 v41, v41\n"
    "v_mfma_f32_16x16x32_bf16 v[12:15], a[116:119], a[20:23], v[12:15]\n"
    "v_exp_f32 v42, v42\n"
    "v_mfma_f32_16x16x32_bf16 v[12:15], a[120:123], a[24:27], v[12:15]\n"
    "v_exp_f32 v43, v43\n"
    "v_mfma_f32_16x16x32_bf16 v[12:15], a[124:127], a[28:31], v[12:15]\n"
    "v_cvt_pk_bf16_f32 v40, v40, v41\n"
    "v_mfma_f32_16x16x32_bf16 v[16:19], a[96:99], a[32:35], v[56:59]\n"
    "v_exp_f32 v44, v44\n"
    "v_mfma_f32_16x16x32_bf16 v[16:19], a[100:103], a[36:39], v[16:19]\n"
    "v_exp_f32 v45, v45\n"
    "v_cvt_pk_bf16_f32 v41, v42, v43\n"
    "v_exp_f32 v46, v46\n"
    "v_exp_f32 v47, v47\n"
    "v_cvt_pk_bf16_f32 v42, v44, v45\n"
    "v_cvt_pk_bf16_f32 v43, v46, v47\n"
    "ds_write_b128 v84, v[40:43] offset:5120\n"
    "v_mfma_f32_16x16x32_bf16 v[16:19], a[104:107], a[40:43], v[16:19]\n"
    "v_mfma_f32_16x16x32_bf16 v[16:19], a[108:111], a[44:47], v[16:19]\n"
    "v_exp_f32 v0, v0\n"
    "s_nop 0\n"
    "v_mfma_f32_16x16x32_bf16 v[112:115], v[72:75], v[32:35], v[112:115]\n"
    "v_mfma_f32_16x16x32_bf16 v[116:119], v[72:75], v[40:43], v[116:119]\n"
    "s_nop 7\n"
    "ds_write_b32 v85, v108 offset:768\n"
    "ds_write_b32 v85, v112 offset:1024\n"
    "ds_write_b32 v85, v116 offset:1280\n"
    "v_mfma_f32_16x16x32_bf16 v[20:23], a[112:115], a[32:35], v[56:59]\n"
    "v_exp_f32 v1, v1\n"
    "v_mfma_f32_16x16x32_bf16 v[20:23], a[116:119], a[36:39], v[20:23]\n"
    "v_exp_f32 v2, v2\n"
    "v_mfma_f32_16x16x32_bf16 v[20:23], a[120:123], a[40:43], v[20:23]\n"
    "v_exp_f32 v3, v3\n"
    "v_mfma_f32_16x16x32_bf16 v[20:23], a[124:127], a[44:47], v[20:23]\n"
    "s_waitcnt lgkmcnt(0)\n"
    "s_barrier\n"
    "v_cvt_pk_bf16_f32 v0, v0, v1\n"
    "v_exp_f32 v4, v4\n"
    "s_setprio 0\n"
    "v_exp_f32 v5, v5\n"
    "ds_read_b128 a[96:99], v83\n"
    "v_cvt_pk_bf16_f32 v1, v2, v3\n"
    "ds_read_b128 a[100:103], v83 offset:512\n"
    "v_exp_f32 v6, v6\n"
    "ds_read_b128 a[104:107], v83 offset:1024\n"
    "v_exp_f32 v7, v7\n"
    "ds_read_b128 a[108:111], v83 offset:1536\n"
    "v_cvt_pk_bf16_f32 v2, v4, v5\n"
    "ds_read_b128 a[112:115], v83 offset:4096\n"
    "v_cvt_pk_bf16_f32 v3, v6, v7\n"
    "ds_read_b128 a[116:119], v83 offset:4608\n"
    "ds_write_b128 v84, v[0:3]\n"
    "v_or3_b32 v87, v0, v1, v2\n"
    "ds_read_b128 a[120:123], v83 offset:5120\n"
    "v_or_b32 v87, v87, v3\n"
    "ds_read_b128 a[124:127], v83 offset:5632\n"
    "v_exp_f32 v8, v8\n"
    "v_exp_f32 v9, v9\n"
    "v_exp_f32 v10, v10\n"
    "v_exp_f32 v11, v11\n"
    "v_cvt_pk_bf16_f32 v8, v8, v9\n"
    "v_exp_f32 v12, v12\n"
    "v_exp_f32 v13, v13\n"
    "v_cvt_pk_bf16_f32 v9, v10, v11\n"
    "v_exp_f32 v14, v14\n"
    "v_exp_f32 v15, v15\n"
    "v_cvt_pk_bf16_f32 v10, v12, v13\n"
    "v_cvt_pk_bf16_f32 v11, v14, v15\n"
    "ds_write_b128 v84, v[8:11] offset:1024\n"
    "v_or3_b32 v87, v87, v8, v9\n"
    "v_or3_b32 v87, v87, v10, v11\n"
    "v_exp_f32 v16, v16\n"
    "v_exp_f32 v17, v17\n"
    "v_exp_f32 v18, v18\n"
    "v_exp_f32 v19, v19\n"
    "v_cvt_pk_bf16_f32 v16, v16, v17\n"
    "v_exp_f32 v20, v20\n"
    "v_exp_f32 v21, v21\n"
    "v_cvt_pk_bf16_f32 v17, v18, v19\n"
    "v_exp_f32 v22, v22\n"
    "v_exp_f32 v23, v23\n"
    "v_cvt_pk_bf16_f32 v18, v20, v21\n"
    "v_cvt_pk_bf16_f32 v19, v22, v23\n"
    "ds_write_b128 v84, v[16:19] offset:2048\n"
    "v_or3_b32 v87, v87, v16, v17\n"
    "v_or3_b32 v87, v87, v18, v19\n"
    "v_xor_b32 v83, 0x8000, v83\n"
    "v_and_b32 v121, 0x40004000, v87\n"
    "v_cmp_ne_u32 vcc, 0, v121\n"
    "s_cbranch_vccnz PSM%=\n"
    "QSM%=:\n"
    "s_setprio 2\n"
    "s_waitcnt lgkmcnt(0)\n"
    "s_barrier\n"
    "SEA%=:\n"
    "ds_read_b32 v120, v81\n"
    "v_mov_b32 v76, v96\n"
    "v_mov_b32 v77, v100\n"
    "v_mov_b32 v78, v104\n"
    "v_mfma_f32_16x16x32_bf16 v[96:99], v[72:75], v[0:3], v[96:99]\n"
    "v_mfma_f32_16x16x32_bf16 v[100:103], v[72:75], v[8:11], v[100:103]\n"
    "v_mfma_f32_16x16x32_bf16 v[104:107], v[72:75], v[16:19], v[104:107]\n"
    "v_mfma_f32_16x16x32_bf16 v[0:3], a[96:99], a[0:3], v[48:51]\n"
    "v_mfma_f32_16x16x32_bf16 v[0:3], a[100:103], a[4:7], v[0:3]\n"
    "v_mfma_f32_16x16x32_bf16 v[0:3], a[104:107], a[8:11], v[0:3]\n"
    "v_mfma_f32_16x16x32_bf16 v[0:3], a[108:111], a[12:15], v[0:3]\n"
    "v_mfma_f32_16x16x32_bf16 v[4:7], a[112:115], a[0:3], v[48:51]\n"
    "v_mfma_f32_16x16x32_bf16 v[4:7], a[116:119], a[4:7], v[4:7]\n"
    "s_waitcnt lgkmcnt(0)\n"
    "v_cmp_eq_u32 vcc, s25, v120\n"
    "s_cbranch_vccnz RSEA%=\n"
    "s_mov_b32 s92, 2\n"
    "s_add_u32 s25, s25, 1\n"
    "s_mov_b32 s29, s89\n"
    "v_mfma_f32_16x16x32_bf16 v[4:7], a[120:123], a[8:11], v[4:7]\n"
    "v_mfma_f32_16x16x32_bf16 v[4:7], a[124:127], a[12:15], v[4:7]\n"
    "v_mfma_f32_16x16x32_bf16 v[8:11], a[96:99], a[16:19], v[52:55]\n"
    "v_mfma_f32_16x16x32_bf16 v[8:11], a[100:103], a[20:23], v[8:11]\n"
    "v_mfma_f32_16x16x32_bf16 v[8:11], a[104:107], a[24:27], v[8:11]\n"
    "v_exp_f32 v0, v0\n"
    "v_mfma_f32_16x16x32_bf16 v[8:11], a[108:111], a[28:31], v[8:11]\n"
    "v_exp_f32 v1, v1\n"
    "v_mfma_f32_16x16x32_bf16 v[12:15], a[112:115], a[16:19], v[52:55]\n"
    "v_exp_f32 v2, v2\n"
    "v_mfma_f32_16x16x32_bf16 v[12:15], a[116:119], a[20:23], v[12:15]\n"
    "v_exp_f32 v3, v3\n"
    "v_mfma_f32_16x16x32_bf16 v[12:15], a[120:123], a[24:27], v[12:15]\n"
    "v_cvt_pk_bf16_f32 v0, v0, v1\n"
    "v_exp_f32 v4, v4\n"
    "v_mfma_f32_16x16x32_bf16 v[12:15], a[124:127], a[28:31], v[12:15]\n"
    "v_exp_f32 v5, v5\n"
    "v_mfma_f32_16x16x32_bf16 v[16:19], a[96:99], a[32:35], v[56:59]\n"
    "v_cvt_pk_bf16_f32 v1, v2, v3\n"
    "v_mfma_f32_16x16x32_bf16 v[16:19], a[100:103], a[36:39], v[16:19]\n"
    "v_exp_f32 v6, v6\n"
    "v_mfma_f32_16x16x32_bf16 v[16:19], a[104:107], a[40:43], v[16:19]\n"
    "v_exp_f32 v7, v7\n"
    "v_mfma_f32_16x16x32_bf16 v[16:19], a[108:111], a[44:47], v[16:19]\n"
    "v_cvt_pk_bf16_f32 v2, v4, v5\n"
    "v_cvt_pk_bf16_f32 v3, v6, v7\n"
    "v_mfma_f32_16x16x32_bf16 v[20:23], a[112:115], a[32:35], v[56:59]\n"
    "v_or3_b32 v87, v0, v1, v2\n"
    "v_or_b32 v87, v87, v3\n"
    "v_mfma_f32_16x16x32_bf16 v[20:23], a[116:119], a[36:39], v[20:23]\n"
    "v_exp_f32 v8, v8\n"
    "v_mfma_f32_16x16x32_bf16 v[20:23], a[120:123], a[40:43], v[20:23]\n"
    "v_exp_f32 v9, v9\n"
    "v_mfma_f32_16x16x32_bf16 v[20:23], a[124:127], a[44:47], v[20:23]\n"
    "s_waitcnt lgkmcnt(0)\n"
    "s_barrier\n"
    "v_exp_f32 v10, v10\n"
    "s_setprio 0\n"
    "ds_write_b128 v84, v[0:3]\n"
    "v_exp_f32 v11, v11\n"
    "v_cvt_pk_bf16_f32 v8, v8, v9\n"
    "v_exp_f32 v12, v12\n"
    "v_exp_f32 v13, v13\n"
    "v_cvt_pk_bf16_f32 v9, v10, v11\n"
    "v_exp_f32 v14, v14\n"
    "v_exp_f32 v15, v15\n"
    "v_cvt_pk_bf16_f32 v10, v12, v13\n"
    "v_cvt_pk_bf16_f32 v11, v14, v15\n"
    "ds_write_b128 v84, v[8:11] offset:1024\n"
    "v_or3_b32 v87, v87, v8, v9\n"
    "v_or3_b32 v87, v87, v10, v11\n"
    "v_exp_f32 v16, v16\n"
    "v_exp_f32 v17, v17\n"
    "v_exp_f32 v18, v18\n"
    "v_exp_f32 v19, v19\n"
    "v_cvt_pk_bf16_f32 v16, v16, v17\n"
    "v_exp_f32 v20, v20\n"
    "v_exp_f32 v21, v21\n"
    "v_cvt_pk_bf16_f32 v17, v18, v19\n"
    "v_exp_f32 v22, v22\n"
    "v_exp_f32 v23, v23\n"
    "v_cvt_pk_bf16_f32 v18, v20, v21\n"
    "v_cvt_pk_bf16_f32 v19, v22, v23\n"
    "ds_write_b128 v84, v[16:19] offset:2048\n"
    "v_or3_b32 v87, v87, v16, v17\n"
    "v_or3_b32 v87, v87, v18, v19\n"
    "v_and_b32 v121, 0x40004000, v87\n"
    "v_cmp_ne_u32 vcc, 0, v121\n"
    "s_cbranch_vccnz PSE%=\n"
    "QSE%=:\n"
    "s_setprio 2\n"
    "v_mfma_f32_16x16x32_bf16 v[4:7], v[72:75], v[0:3], v[96:99]\n"
    "v_mfma_f32_16x16x32_bf16 v[12:15], v[72:75], v[8:11], v[100:103]\n"
    "v_mfma_f32_16x16x32_bf16 v[20:23], v[72:75], v[16:19], v[104:107]\n"
    "s_nop 5\n"
    "ds_write_b32 v85, v4\n"
    "ds_write_b32 v85, v12 offset:256\n"
    "ds_write_b32 v85, v20 offset:512\n"
    "s_waitcnt lgkmcnt(0)\n"
    "s_barrier\n"
    "SENDA%=:\n"
    "ds_read_b32 v120, v81\n"
    "s_waitcnt lgkmcnt(0)\n"
    "v_cmp_eq_u32 vcc, s25, v120\n"
    "s_cbranch_vccnz RSENDA%=\n"
    "s_endpgm\n"
    "OWAVE%=:\n"
    "v_mbcnt_lo_u32_b32 v82, -1, 0\n"
    "s_and_b32 s56, %1, 7\n"
    "v_mbcnt_hi_u32_b32 v82, -1, v82\n"
    "s_lshr_b32 s57, %1, 5\n"
    "s_lshl3_add_u32 s56, s56, s57\n"
    "v_bfe_u32 v0, v82, 2, 3\n"
    "v_lshrrev_b32 v1, 3, v82\n"
    "v_and_b32 v1, 2, v1\n"
    "v_and_b32 v2, 3, v82\n"
    "v_xor_b32 v2, v2, v1\n"
    "v_lshrrev_b32 v3, 5, v82\n"
    "v_lshlrev_b32 v2, 4, v2\n"
    "v_lshl_or_b32 v2, v0, 8, v2\n"
    "v_lshl_or_b32 v80, v3, 6, v2\n"
    "s_bfe_u32 s57, %1, 0x20003\n"
    "s_and_b32 s20, %2, 3\n"
    "s_getpc_b64 s[64:65]\n"
    "PCREF%=:\n"
    "s_sub_u32 s64, s64, PCREF%=-KSTART%=\n"
    "s_subb_u32 s65, s65, 0\n"
    "s_and_b32 s65, s65, 0xffff\n"
    "s_mov_b32 s66, KEND%=-KSTART%=\n"
    "s_mov_b32 s67, 0x20000\n"
    "s_bfe_u32 s68, %1, 0x40003\n"
    "s_lshl2_add_u32 s68, s68, s20\n"
    "s_mul_i32 s68, s68, 256\n"
    "v_lshlrev_b32 v60, 7, v82\n"
    "v_mov_b32 v61, 0x7ff00000\n"
    "v_cmp_gt_u32 vcc, 2, v82\n"
    "s_nop 1\n"
    "v_cndmask_b32 v60, v61, v60, vcc\n"
    "buffer_load_dword v61, v60, s[64:67], s68 offen\n"
    "s_and_b32 s58, s57, 1\n"
    "s_lshl_b32 s23, s20, 13\n"
    "s_mul_i32 s21, s58, 229376\n"
    "s_lshr_b32 s61, s56, 3\n"
    "s_mul_i32 s61, s61, 229376\n"
    "s_add_u32 s21, s21, s61\n"
    "s_and_b32 s21, s21, 0x78000\n"
    "s_lshl_b32 s62, s56, 19\n"
    "s_add_u32 s62, s62, s23\n"
    "s_add_u32 s21, s21, s62\n"
    "s_add_u32 s96, s21, 0x8000\n"
    "s_and_b32 s71, s96, 0x78000\n"
    "s_cselect_b32 s71, 0, 0x80000\n"
    "s_sub_u32 s96, s96, s71\n"
    "s_lshl_b32 s89, s58, 15\n"
    "s_add_u32 s93, s61, 458752\n"
    "s_add_u32 s60, s61, 491520\n"
    "s_add_u32 s93, s93, s89\n"
    "s_sub_u32 s89, s60, s89\n"
    "s_and_b32 s93, s93, 0x78000\n"
    "s_add_u32 s93, s93, s62\n"
    "s_and_b32 s89, s89, 0x78000\n"
    "s_add_u32 s89, s89, s62\n"
    "s_mov_b32 s75, 0xff61b1e6\n"
    "s_mov_b32 s55, %2\n"
    "v_mov_b32 v81, 0x26c00\n"
    "s_lshl2_add_u32 s59, %1, s20\n"
    "s_xor_b32 s60, s59, 32\n"
    "s_lshl_b32 s91, s59, 7\n"
    "s_lshl_b32 s92, s60, 7\n"
    "s_lshl_b32 s79, s59, 12\n"
    "s_add_u32 s79, s79, 0x100000\n"
    "s_lshl_b32 s78, s60, 12\n"
    "s_add_u32 s78, s78, 0x100000\n"
    "s_add_u32 s24, s23, 0x10000\n"
    "s_mov_b32 s22, s21\n"
    "s_xor_b32 s23, s23, 0x8000\n"
    "s_lshl2_add_u32 s94, s56, s57\n"
    "s_mul_i32 s94, s94, 12288\n"
    "v_lshlrev_b32 v16, 6, v0\n"
    "v_bfe_u32 v18, v82, 1, 1\n"
    "v_xor_b32 v18, v18, v1\n"
    "v_lshl_or_b32 v16, v18, 4, v16\n"
    "v_and_b32 v18, 1, v82\n"
    "v_lshl_or_b32 v16, v18, 3, v16\n"
    "v_lshl_or_b32 v16, v3, 11, v16\n"
    "s_lshl_b32 s69, s20, 9\n"
    "s_add_u32 s69, s69, 0x10000\n"
    "v_add_u32 v84, s69, v16\n"
    "v_xor_b32 v85, 32, v84\n"
    "v_lshlrev_b32 v87, 4, v82\n"
    "v_add_u32 v86, 0x20000, v87\n"
    "v_and_b32 v90, 31, v82\n"
    "v_lshlrev_b32 v90, 2, v90\n"
    "v_and_b32 v88, 0xf0, v87\n"
    "v_add_u32 v88, 0x26600, v88\n"
    "s_mov_b32 s90, 0\n"
    "s_mov_b32 s25, 2\n"
    "s_mov_b32 s26, 0\n"
    "v_mov_b32 v64, 0x42000000\n"
    "v_mov_b32 v65, 0x42000000\n"
    "v_mov_b32 v66, 0x42000000\n"
    "v_mov_b32 v67, 0x42000000\n"
    "v_mov_b32 v68, 0x42000000\n"
    "v_mov_b32 v69, 0x42000000\n"
    "v_mov_b64 v[0:1], 0\n"
    "v_mov_b64 v[2:3], 0\n"
    "v_mov_b64 v[4:5], 0\n"
    "v_mov_b64 v[6:7], 0\n"
    "v_mov_b64 v[8:9], 0\n"
    "v_mov_b64 v[10:11], 0\n"
    "v_mov_b64 v[12:13], 0\n"
    "v_mov_b64 v[14:15], 0\n"
    "v_mov_b64 v[16:17], 0\n"
    "v_mov_b64 v[18:19], 0\n"
    "v_mov_b64 v[20:21], 0\n"
    "v_mov_b64 v[22:23], 0\n"
    "v_mov_b64 v[24:25], 0\n"
    "v_mov_b64 v[26:27], 0\n"
    "v_mov_b64 v[28:29], 0\n"
    "v_mov_b64 v[30:31], 0\n"
    "v_mov_b64 v[32:33], 0\n"
    "v_mov_b64 v[34:35], 0\n"
    "v_mov_b64 v[36:37], 0\n"
    "v_mov_b64 v[38:39], 0\n"
    "v_mov_b64 v[40:41], 0\n"
    "v_mov_b64 v[42:43], 0\n"
    "v_mov_b64 v[44:45], 0\n"
    "v_mov_b64 v[46:47], 0\n"
    "s_waitcnt lgkmcnt(0)\n"
    "s_barrier\n"
    "buffer_load_dword v91, off, s[80:83], s91 sc1\n"
    "s_mov_b32 s21, s96\n"
    "s_mov_b32 m0, s23\n"
    "s_add_u32 s54, s21, 0x1000\n"
    "buffer_load_dwordx4 v80, s[36:39], s21 offen lds\n"
    "buffer_load_dwordx4 v80, s[36:39], s21 offen offset:2048 lds\n"
    "s_add_u32 m0, s23, 896\n"
    "s_nop 0\n"
    "buffer_load_dwordx4 v80, s[36:39], s21 offen offset:128 lds\n"
    "buffer_load_dwordx4 v80, s[36:39], s21 offen offset:2176 lds\n"
    "s_add_u32 m0, s23, 0x1000\n"
    "s_add_u32 s21, s21, 0x8000\n"
    "buffer_load_dwordx4 v80, s[36:39], s54 offen lds\n"
    "buffer_load_dwordx4 v80, s[36:39], s54 offen offset:2048 lds\n"
    "s_add_u32 m0, s23, 4992\n"
    "s_xor_b32 s23, s23, 0x8000\n"
    "buffer_load_dwordx4 v80, s[36:39], s54 offen offset:128 lds\n"
    "buffer_load_dwordx4 v80, s[36:39], s54 offen offset:2176 lds\n"
    "s_waitcnt vmcnt(8)\n"
    "v_readfirstlane_b32 s27, v91\n"
    "s_add_u32 s27, s27, 1\n"
    "s_nop 0\n"
    "v_mov_b32 v91, s27\n"
    "s_add_u32 s69, s91, 0x20000\n"
    "buffer_store_dword v91, v90, s[80:83], s69 offen sc1\n"
    "s_barrier\n"
    "s_mov_b32 m0, s24\n"
    "s_add_u32 s54, s22, 0x1000\n"
    "buffer_load_dwordx4 v80, s[40:43], s22 offen lds\n"
    "buffer_load_dwordx4 v80, s[40:43], s22 offen offset:2048 lds\n"
    "s_add_u32 m0, s24, 896\n"
    "s_nop 0\n"
    "buffer_load_dwordx4 v80, s[40:43], s22 offen offset:128 lds\n"
    "buffer_load_dwordx4 v80, s[40:43], s22 offen offset:2176 lds\n"
    "s_add_u32 m0, s24, 0x1000\n"
    "s_add_u32 s22, s22, 0x8000\n"
    "buffer_load_dwordx4 v80, s[40:43], s54 offen lds\n"
    "buffer_load_dwordx4 v80, s[40:43], s54 offen offset:2048 lds\n"
    "s_add_u32 m0, s24, 4992\n"
    "s_xor_b32 s24, s24, 0x8000\n"
    "buffer_load_dwordx4 v80, s[40:43], s54 offen offset:128 lds\n"
    "buffer_load_dwordx4 v80, s[40:43], s54 offen offset:2176 lds\n"
    "s_mov_b32 s22, s96\n"
    "s_mov_b32 m0, s24\n"
    "s_add_u32 s54, s22, 0x1000\n"
    "buffer_load_dwordx4 v80, s[40:43], s22 offen lds\n"
    "buffer_load_dwordx4 v80, s[40:43], s22 offen offset:2048 lds\n"
    "s_add_u32 m0, s24, 896\n"
    "s_nop 0\n"
    "buffer_load_dwordx4 v80, s[40:43], s22 offen offset:128 lds\n"
    "buffer_load_dwordx4 v80, s[40:43], s22 offen offset:2176 lds\n"
    "s_add_u32 m0, s24, 0x1000\n"
    "s_add_u32 s22, s22, 0x8000\n"
    "buffer_load_dwordx4 v80, s[40:43], s54 offen lds\n"
    "buffer_load_dwordx4 v80, s[40:43], s54 offen offset:2048 lds\n"
    "s_add_u32 m0, s24, 4992\n"
    "s_xor_b32 s24, s24, 0x8000\n"
    "buffer_load_dwordx4 v80, s[40:43], s54 offen offset:128 lds\n"
    "buffer_load_dwordx4 v80, s[40:43], s54 offen offset:2176 lds\n"
    "s_waitcnt vmcnt(16)\n"
    "s_barrier\n"
    "s_and_b32 s71, s21, 0x78000\n"
    "s_cselect_b32 s71, 0, 0x80000\n"
    "s_sub_u32 s21, s21, s71\n"
    "s_mov_b32 m0, s23\n"
    "s_add_u32 s54, s21, 0x1000\n"
    "buffer_load_dwordx4 v80, s[36:39], s21 offen lds\n"
    "buffer_load_dwordx4 v80, s[36:39], s21 offen offset:2048 lds\n"
    "s_add_u32 m0, s23, 896\n"
    "s_nop 0\n"
    "buffer_load_dwordx4 v80, s[36:39], s21 offen offset:128 lds\n"
    "buffer_load_dwordx4 v80, s[36:39], s21 offen offset:2176 lds\n"
    "s_add_u32 m0, s23, 0x1000\n"
    "s_add_u32 s21, s21, 0x8000\n"
    "buffer_load_dwordx4 v80, s[36:39], s54 offen lds\n"
    "buffer_load_dwordx4 v80, s[36:39], s54 offen offset:2048 lds\n"
    "s_add_u32 m0, s23, 4992\n"
    "s_xor_b32 s23, s23, 0x8000\n"
    "buffer_load_dwordx4 v80, s[36:39], s54 offen offset:128 lds\n"
    "buffer_load_dwordx4 v80, s[36:39], s54 offen offset:2176 lds\n"
    "s_setprio 1\n"
    "s_waitcnt vmcnt(16)\n"
    "OL%=:\n"
    "s_barrier\n"
    "OLA%=:\n"
    "ds_read_b32 v120, v81\n"
    "ds_read_b64_tr_b16 a[96:97], v84\n"
    "ds_read_b64_tr_b16 a[98:99], v84 offset:4096\n"
    "ds_read_b128 a[0:3], v86\n"
    "ds_read_b128 a[4:7], v86 offset:1024\n"
    "s_waitcnt lgkmcnt(4)\n"
    "v_cmp_eq_u32 vcc, s25, v120\n"
    "s_cbranch_vccnz ROLA%=\n"
    "s_add_u32 s25, s25, 1\n"
    "s_mov_b32 s75, 0\n"
    "s_and_b32 s71, s21, 0x78000\n"
    "s_cselect_b32 s71, 0, 0x80000\n"
    "s_sub_u32 s21, s21, s71\n"
    "s_cmp_eq_u32 s26, 4\n"
    "s_cselect_b32 s21, s93, s21\n"
    "s_mov_b32 m0, s23\n"
    "s_add_u32 s54, s21, 0x1000\n"
    "buffer_load_dwordx4 v80, s[36:39], s21 offen lds\n"
    "buffer_load_dwordx4 v80, s[36:39], s21 offen offset:2048 lds\n"
    "ds_read_b64_tr_b16 a[100:101], v85\n"
    "ds_read_b64_tr_b16 a[102:103], v85 offset:4096\n"
    "ds_read_b128 a[8:11], v86 offset:2048\n"
    "ds_read_b128 a[12:15], v86 offset:3072\n"
    "s_add_u32 m0, s23, 896\n"
    "s_nop 0\n"
    "buffer_load_dwordx4 v80, s[36:39], s21 offen offset:128 lds\n"
    "buffer_load_dwordx4 v80, s[36:39], s21 offen offset:2176 lds\n"
    "ds_read_b64_tr_b16 a[104:105], v84 offset:8192\n"
    "ds_read_b64_tr_b16 a[106:107], v84 offset:12288\n"
    "ds_read_b128 a[24:27], v86 offset:6144\n"
    "ds_read_b128 a[28:31], v86 offset:7168\n"
    "s_add_u32 m0, s23, 0x1000\n"
    "s_add_u32 s21, s21, 0x8000\n"
    "buffer_load_dwordx4 v80, s[36:39], s54 offen lds\n"
    "buffer_load_dwordx4 v80, s[36:39], s54 offen offset:2048 lds\n"
    "s_waitcnt lgkmcnt(8)\n"
    "ds_read_b64_tr_b16 a[108:109], v85 offset:8192\n"
    "ds_read_b64_tr_b16 a[110:111], v85 offset:12288\n"
    "ds_read_b128 a[32:35], v86 offset:8192\n"
    "ds_read_b128 a[36:39], v86 offset:9216\n"
    "s_add_u32 m0, s23, 4992\n"
    "s_xor_b32 s23, s23, 0x8000\n"
    "buffer_load_dwordx4 v80, s[36:39], s54 offen offset:128 lds\n"
    "buffer_load_dwordx4 v80, s[36:39], s54 offen offset:2176 lds\n"
    "s_waitcnt lgkmcnt(8)\n"
    "ds_read_b64_tr_b16 a[112:113], v84 offset:16384\n"
    "ds_read_b64_tr_b16 a[114:115], v84 offset:20480\n"
    "ds_read_b128 a[48:51], v86 offset:12288\n"
    "ds_read_b128 a[52:55], v86 offset:13312\n"
    "s_waitcnt lgkmcnt(8)\n"
    "ds_read_b64_tr_b16 a[116:117], v85 offset:16384\n"
    "ds_read_b64_tr_b16 a[118:119], v85 offset:20480\n"
    "ds_read_b128 a[56:59], v86 offset:14336\n"
    "ds_read_b128 a[60:63], v86 offset:15360\n"
    "s_waitcnt lgkmcnt(8)\n"
    "ds_read_b64_tr_b16 a[120:121], v84 offset:24576\n"
    "ds_read_b64_tr_b16 a[122:123], v84 offset:28672\n"
    "ds_read_b128 a[72:75], v86 offset:18432\n"
    "ds_read_b128 a[76:79], v86 offset:19456\n"
    "s_waitcnt lgkmcnt(8)\n"
    "ds_read_b64_tr_b16 a[124:125], v85 offset:24576\n"
    "ds_read_b64_tr_b16 a[126:127], v85 offset:28672\n"
    "ds_read_b128 a[80:83], v86 offset:20480\n"
    "ds_read_b128 a[84:87], v86 offset:21504\n"
    "v_xor_b32 v84, 0x8000, v84\n"
    "v_xor_b32 v85, 0x8000, v85\n"
    "s_cmp_eq_u32 s26, 0\n"
    "s_cbranch_scc1 OW0%=\n"
    "OW1%=:\n"
    "s_waitcnt vmcnt(16) lgkmcnt(0)\n"
    "s_barrier\n"
    "v_mfma_f32_16x16x32_bf16 v[0:3], a[96:99], a[0:3], v[0:3]\n"
    "ds_read_b128 a[16:19], v86 offset:4096\n"
    "s_and_b32 s71, s22, 0x78000\n"
    "s_cselect_b32 s71, 0, 0x80000\n"
    "s_sub_u32 s22, s22, s71\n"
    "s_mov_b32 m0, s24\n"
    "s_add_u32 s54, s22, 0x1000\n"
    "buffer_load_dwordx4 v80, s[40:43], s22 offen lds\n"
    "v_mfma_f32_16x16x32_bf16 v[0:3], a[104:107], a[24:27], v[0:3]\n"
    "ds_read_b128 a[40:43], v86 offset:10240\n"
    "v_mfma_f32_16x16x32_bf16 v[0:3], a[112:115], a[48:51], v[0:3]\n"
    "ds_read_b128 a[64:67], v86 offset:16384\n"
    "v_mfma_f32_16x16x32_bf16 v[0:3], a[120:123], a[72:75], v[0:3]\n"
    "ds_read_b128 a[88:91], v86 offset:22528\n"
    "v_mfma_f32_16x16x32_bf16 v[4:7], a[100:103], a[0:3], v[4:7]\n"
    "ds_read_b128 a[20:23], v86 offset:5120\n"
    "v_mfma_f32_16x16x32_bf16 v[4:7], a[108:111], a[24:27], v[4:7]\n"
    "ds_read_b128 a[44:47], v86 offset:11264\n"
    "buffer_load_dwordx4 v80, s[40:43], s22 offen offset:2048 lds\n"
    "v_mfma_f32_16x16x32_bf16 v[4:7], a[116:119], a[48:51], v[4:7]\n"
    "ds_read_b128 a[68:71], v86 offset:17408\n"
    "v_mfma_f32_16x16x32_bf16 v[4:7], a[124:127], a[72:75], v[4:7]\n"
    "ds_read_b128 a[92:95], v86 offset:23552\n"
    "v_mfma_f32_16x16x32_bf16 v[8:11], a[96:99], a[4:7], v[8:11]\n"
    "v_mfma_f32_16x16x32_bf16 v[8:11], a[104:107], a[28:31], v[8:11]\n"
    "v_mfma_f32_16x16x32_bf16 v[8:11], a[112:115], a[52:55], v[8:11]\n"
    "v_mfma_f32_16x16x32_bf16 v[8:11], a[120:123], a[76:79], v[8:11]\n"
    "s_add_u32 m0, s24, 896\n"
    "s_nop 0\n"
    "buffer_load_dwordx4 v80, s[40:43], s22 offen offset:128 lds\n"
    "v_mfma_f32_16x16x32_bf16 v[12:15], a[100:103], a[4:7], v[12:15]\n"
    "v_mfma_f32_16x16x32_bf16 v[12:15], a[108:111], a[28:31], v[12:15]\n"
    "v_mfma_f32_16x16x32_bf16 v[12:15], a[116:119], a[52:55], v[12:15]\n"
    "v_mfma_f32_16x16x32_bf16 v[12:15], a[124:127], a[76:79], v[12:15]\n"
    "v_mfma_f32_16x16x32_bf16 v[16:19], a[96:99], a[8:11], v[16:19]\n"
    "buffer_load_dwordx4 v80, s[40:43], s22 offen offset:2176 lds\n"
    "v_mfma_f32_16x16x32_bf16 v[16:19], a[104:107], a[32:35], v[16:19]\n"
    "v_mfma_f32_16x16x32_bf16 v[16:19], a[112:115], a[56:59], v[16:19]\n"
    "v_mfma_f32_16x16x32_bf16 v[16:19], a[120:123], a[80:83], v[16:19]\n"
    "v_mfma_f32_16x16x32_bf16 v[20:23], a[100:103], a[8:11], v[20:23]\n"
    "v_mfma_f32_16x16x32_bf16 v[20:23], a[108:111], a[32:35], v[20:23]\n"
    "v_mfma_f32_16x16x32_bf16 v[20:23], a[116:119], a[56:59], v[20:23]\n"
    "s_add_u32 m0, s24, 0x1000\n"
    "s_add_u32 s22, s22, 0x8000\n"
    "buffer_load_dwordx4 v80, s[40:43], s54 offen lds\n"
    "v_mfma_f32_16x16x32_bf16 v[20:23], a[124:127], a[80:83], v[20:23]\n"
    "v_mfma_f32_16x16x32_bf16 v[24:27], a[96:99], a[12:15], v[24:27]\n"
    "v_mfma_f32_16x16x32_bf16 v[24:27], a[104:107], a[36:39], v[24:27]\n"
    "v_mfma_f32_16x16x32_bf16 v[24:27], a[112:115], a[60:63], v[24:27]\n"
    "v_mfma_f32_16x16x32_bf16 v[24:27], a[120:123], a[84:87], v[24:27]\n"
    "v_mfma_f32_16x16x32_bf16 v[28:31], a[100:103], a[12:15], v[28:31]\n"
    "buffer_load_dwordx4 v80, s[40:43], s54 offen offset:2048 lds\n"
    "v_mfma_f32_16x16x32_bf16 v[28:31], a[108:111], a[36:39], v[28:31]\n"
    "v_mfma_f32_16x16x32_bf16 v[28:31], a[116:119], a[60:63], v[28:31]\n"
    "v_mfma_f32_16x16x32_bf16 v[28:31], a[124:127], a[84:87], v[28:31]\n"
    "s_waitcnt lgkmcnt(0)\n"
    "v_mfma_f32_16x16x32_bf16 v[32:35], a[96:99], a[16:19], v[32:35]\n"
    "v_mfma_f32_16x16x32_bf16 v[32:35], a[104:107], a[40:43], v[32:35]\n"
    "s_add_u32 m0, s24, 4992\n"
    "s_xor_b32 s24, s24, 0x8000\n"
    "buffer_load_dwordx4 v80, s[40:43], s54 offen offset:128 lds\n"
    "v_mfma_f32_16x16x32_bf16 v[32:35], a[112:115], a[64:67], v[32:35]\n"
    "v_mfma_f32_16x16x32_bf16 v[32:35], a[120:123], a[88:91], v[32:35]\n"
    "v_mfma_f32_16x16x32_bf16 v[36:39], a[100:103], a[16:19], v[36:39]\n"
    "v_mfma_f32_16x16x32_bf16 v[36:39], a[108:111], a[40:43], v[36:39]\n"
    "v_mfma_f32_16x16x32_bf16 v[36:39], a[116:119], a[64:67], v[36:39]\n"
    "v_mfma_f32_16x16x32_bf16 v[36:39], a[124:127], a[88:91], v[36:39]\n"
    "buffer_load_dwordx4 v80, s[40:43], s54 offen offset:2176 lds\n"
    "v_mfma_f32_16x16x32_bf16 v[40:43], a[96:99], a[20:23], v[40:43]\n"
    "v_mfma_f32_16x16x32_bf16 v[40:43], a[104:107], a[44:47], v[40:43]\n"
    "v_mfma_f32_16x16x32_bf16 v[40:43], a[112:115], a[68:71], v[40:43]\n"
    "v_mfma_f32_16x16x32_bf16 v[40:43], a[120:123], a[92:95], v[40:43]\n"
    "v_mfma_f32_16x16x32_bf16 v[44:47], a[100:103], a[20:23], v[44:47]\n"
    "v_mfma_f32_16x16x32_bf16 v[44:47], a[108:111], a[44:47], v[44:47]\n"
    "v_mfma_f32_16x16x32_bf16 v[44:47], a[116:119], a[68:71], v[44:47]\n"
    "v_mfma_f32_16x16x32_bf16 v[44:47], a[124:127], a[92:95], v[44:47]\n"
    "s_cmp_lt_u32 s26, 2\n"
    "s_cbranch_scc1 PE0%=\n"
    "PE1%=:\n"
    "s_waitcnt vmcnt(16)\n"
    "s_add_u32 s26, s26, 1\n"
    "s_cmp_lt_u32 s26, 5\n"
    "s_cbranch_scc1 OL%=\n"
    "s_barrier\n"
    "O5A%=:\n"
    "ds_read_b32 v120, v81\n"
    "ds_read_b64_tr_b16 a[96:97], v84\n"
    "ds_read_b64_tr_b16 a[98:99], v84 offset:4096\n"
    "ds_read_b128 a[0:3], v86\n"
    "ds_read_b128 a[4:7], v86 offset:1024\n"
    "s_waitcnt lgkmcnt(4)\n"
    "v_cmp_eq_u32 vcc, s25, v120\n"
    "s_cbranch_vccnz RO5A%=\n"
    "s_add_u32 s25, s25, 1\n"
    "s_mov_b32 s75, 0\n"
    "s_mov_b32 s21, s89\n"
    "s_mov_b32 m0, s23\n"
    "s_add_u32 s54, s21, 0x1000\n"
    "buffer_load_dwordx4 v80, s[36:39], s21 offen lds\n"
    "buffer_load_dwordx4 v80, s[36:39], s21 offen offset:2048 lds\n"
    "ds_read_b64_tr_b16 a[100:101], v85\n"
    "ds_read_b64_tr_b16 a[102:103], v85 offset:4096\n"
    "ds_read_b128 a[8:11], v86 offset:2048\n"
    "ds_read_b128 a[12:15], v86 offset:3072\n"
    "s_add_u32 m0, s23, 896\n"
    "s_nop 0\n"
    "buffer_load_dwordx4 v80, s[36:39], s21 offen offset:128 lds\n"
    "buffer_load_dwordx4 v80, s[36:39], s21 offen offset:2176 lds\n"
    "ds_read_b64_tr_b16 a[104:105], v84 offset:8192\n"
    "ds_read_b64_tr_b16 a[106:107], v84 offset:12288\n"
    "ds_read_b128 a[24:27], v86 offset:6144\n"
    "ds_read_b128 a[28:31], v86 offset:7168\n"
    "s_add_u32 m0, s23, 0x1000\n"
    "s_add_u32 s21, s21, 0x8000\n"
    "buffer_load_dwordx4 v80, s[36:39], s54 offen lds\n"
    "buffer_load_dwordx4 v80, s[36:39], s54 offen offset:2048 lds\n"
    "s_waitcnt lgkmcnt(8)\n"
    "ds_read_b64_tr_b16 a[108:109], v85 offset:8192\n"
    "ds_read_b64_tr_b16 a[110:111], v85 offset:12288\n"
    "ds_read_b128 a[32:35], v86 offset:8192\n"
    "ds_read_b128 a[36:39], v86 offset:9216\n"
    "s_add_u32 m0, s23, 4992\n"
    "s_xor_b32 s23, s23, 0x8000\n"
    "buffer_load_dwordx4 v80, s[36:39], s54 offen offset:128 lds\n"
    "buffer_load_dwordx4 v80, s[36:39], s54 offen offset:2176 lds\n"
    "s_waitcnt lgkmcnt(8)\n"
    "ds_read_b64_tr_b16 a[112:113], v84 offset:16384\n"
    "ds_read_b64_tr_b16 a[114:115], v84 offset:20480\n"
    "ds_read_b128 a[48:51], v86 offset:12288\n"
    "ds_read_b128 a[52:55], v86 offset:13312\n"
    "s_waitcnt lgkmcnt(8)\n"
    "ds_read_b64_tr_b16 a[116:117], v85 offset:16384\n"
    "ds_read_b64_tr_b16 a[118:119], v85 offset:20480\n"
    "ds_read_b128 a[56:59], v86 offset:14336\n"
    "ds_read_b128 a[60:63], v86 offset:15360\n"
    "s_waitcnt lgkmcnt(8)\n"
    "ds_read_b64_tr_b16 a[120:121], v84 offset:24576\n"
    "ds_read_b64_tr_b16 a[122:123], v84 offset:28672\n"
    "ds_read_b128 a[72:75], v86 offset:18432\n"
    "ds_read_b128 a[76:79], v86 offset:19456\n"
    "s_waitcnt lgkmcnt(8)\n"
    "ds_read_b64_tr_b16 a[124:125], v85 offset:24576\n"
    "ds_read_b64_tr_b16 a[126:127], v85 offset:28672\n"
    "ds_read_b128 a[80:83], v86 offset:20480\n"
    "ds_read_b128 a[84:87], v86 offset:21504\n"
    "v_xor_b32 v84, 0x8000, v84\n"
    "v_xor_b32 v85, 0x8000, v85\n"
    "s_waitcnt vmcnt(16) lgkmcnt(0)\n"
    "s_barrier\n"
    "v_mfma_f32_16x16x32_bf16 v[0:3], a[96:99], a[0:3], v[0:3]\n"
    "ds_read_b128 a[16:19], v86 offset:4096\n"
    "s_mov_b32 s22, s93\n"
    "s_mov_b32 m0, s24\n"
    "s_add_u32 s54, s22, 0x1000\n"
    "buffer_load_dwordx4 v80, s[40:43], s22 offen lds\n"
    "v_mfma_f32_16x16x32_bf16 v[0:3], a[104:107], a[24:27], v[0:3]\n"
    "ds_read_b128 a[40:43], v86 offset:10240\n"
    "v_mfma_f32_16x16x32_bf16 v[0:3], a[112:115], a[48:51], v[0:3]\n"
    "ds_read_b128 a[64:67], v86 offset:16384\n"
    "v_mfma_f32_16x16x32_bf16 v[0:3], a[120:123], a[72:75], v[0:3]\n"
    "ds_read_b128 a[88:91], v86 offset:22528\n"
    "v_mfma_f32_16x16x32_bf16 v[4:7], a[100:103], a[0:3], v[4:7]\n"
    "ds_read_b128 a[20:23], v86 offset:5120\n"
    "v_mfma_f32_16x16x32_bf16 v[4:7], a[108:111], a[24:27], v[4:7]\n"
    "ds_read_b128 a[44:47], v86 offset:11264\n"
    "buffer_load_dwordx4 v80, s[40:43], s22 offen offset:2048 lds\n"
    "v_mfma_f32_16x16x32_bf16 v[4:7], a[116:119], a[48:51], v[4:7]\n"
    "ds_read_b128 a[68:71], v86 offset:17408\n"
    "v_mfma_f32_16x16x32_bf16 v[4:7], a[124:127], a[72:75], v[4:7]\n"
    "ds_read_b128 a[92:95], v86 offset:23552\n"
    "v_mfma_f32_16x16x32_bf16 v[8:11], a[96:99], a[4:7], v[8:11]\n"
    "v_mfma_f32_16x16x32_bf16 v[8:11], a[104:107], a[28:31], v[8:11]\n"
    "v_mfma_f32_16x16x32_bf16 v[8:11], a[112:115], a[52:55], v[8:11]\n"
    "v_mfma_f32_16x16x32_bf16 v[8:11], a[120:123], a[76:79], v[8:11]\n"
    "s_add_u32 m0, s24, 896\n"
    "s_nop 0\n"
    "buffer_load_dwordx4 v80, s[40:43], s22 offen offset:128 lds\n"
    "v_mfma_f32_16x16x32_bf16 v[12:15], a[100:103], a[4:7], v[12:15]\n"
    "v_mfma_f32_16x16x32_bf16 v[12:15], a[108:111], a[28:31], v[12:15]\n"
    "v_mfma_f32_16x16x32_bf16 v[12:15], a[116:119], a[52:55], v[12:15]\n"
    "v_mfma_f32_16x16x32_bf16 v[12:15], a[124:127], a[76:79], v[12:15]\n"
    "v_mfma_f32_16x16x32_bf16 v[16:19], a[96:99], a[8:11], v[16:19]\n"
    "buffer_load_dwordx4 v80, s[40:43], s22 offen offset:2176 lds\n"
    "v_mfma_f32_16x16x32_bf16 v[16:19], a[104:107], a[32:35], v[16:19]\n"
    "v_mfma_f32_16x16x32_bf16 v[16:19], a[112:115], a[56:59], v[16:19]\n"
    "v_mfma_f32_16x16x32_bf16 v[16:19], a[120:123], a[80:83], v[16:19]\n"
    "v_mfma_f32_16x16x32_bf16 v[20:23], a[100:103], a[8:11], v[20:23]\n"
    "v_mfma_f32_16x16x32_bf16 v[20:23], a[108:111], a[32:35], v[20:23]\n"
    "v_mfma_f32_16x16x32_bf16 v[20:23], a[116:119], a[56:59], v[20:23]\n"
    "s_add_u32 m0, s24, 0x1000\n"
    "s_add_u32 s22, s22, 0x8000\n"
    "buffer_load_dwordx4 v80, s[40:43], s54 offen lds\n"
    "v_mfma_f32_16x16x32_bf16 v[20:23], a[124:127], a[80:83], v[20:23]\n"
    "v_mfma_f32_16x16x32_bf16 v[24:27], a[96:99], a[12:15], v[24:27]\n"
    "v_mfma_f32_16x16x32_bf16 v[24:27], a[104:107], a[36:39], v[24:27]\n"
    "v_mfma_f32_16x16x32_bf16 v[24:27], a[112:115], a[60:63], v[24:27]\n"
    "v_mfma_f32_16x16x32_bf16 v[24:27], a[120:123], a[84:87], v[24:27]\n"
    "v_mfma_f32_16x16x32_bf16 v[28:31], a[100:103], a[12:15], v[28:31]\n"
    "buffer_load_dwordx4 v80, s[40:43], s54 offen offset:2048 lds\n"
    "v_mfma_f32_16x16x32_bf16 v[28:31], a[108:111], a[36:39], v[28:31]\n"
    "v_mfma_f32_16x16x32_bf16 v[28:31], a[116:119], a[60:63], v[28:31]\n"
    "v_mfma_f32_16x16x32_bf16 v[28:31], a[124:127], a[84:87], v[28:31]\n"
    "s_waitcnt lgkmcnt(0)\n"
    "v_mfma_f32_16x16x32_bf16 v[32:35], a[96:99], a[16:19], v[32:35]\n"
    "v_mfma_f32_16x16x32_bf16 v[32:35], a[104:107], a[40:43], v[32:35]\n"
    "s_add_u32 m0, s24, 4992\n"
    "s_xor_b32 s24, s24, 0x8000\n"
    "buffer_load_dwordx4 v80, s[40:43], s54 offen offset:128 lds\n"
    "v_mfma_f32_16x16x32_bf16 v[32:35], a[112:115], a[64:67], v[32:35]\n"
    "v_mfma_f32_16x16x32_bf16 v[32:35], a[120:123], a[88:91], v[32:35]\n"
    "v_mfma_f32_16x16x32_bf16 v[36:39], a[100:103], a[16:19], v[36:39]\n"
    "v_mfma_f32_16x16x32_bf16 v[36:39], a[108:111], a[40:43], v[36:39]\n"
    "v_mfma_f32_16x16x32_bf16 v[36:39], a[116:119], a[64:67], v[36:39]\n"
    "v_mfma_f32_16x16x32_bf16 v[36:39], a[124:127], a[88:91], v[36:39]\n"
    "buffer_load_dwordx4 v80, s[40:43], s54 offen offset:2176 lds\n"
    "v_mfma_f32_16x16x32_bf16 v[40:43], a[96:99], a[20:23], v[40:43]\n"
    "v_mfma_f32_16x16x32_bf16 v[40:43], a[104:107], a[44:47], v[40:43]\n"
    "v_mfma_f32_16x16x32_bf16 v[40:43], a[112:115], a[68:71], v[40:43]\n"
    "v_mfma_f32_16x16x32_bf16 v[40:43], a[120:123], a[92:95], v[40:43]\n"
    "v_mfma_f32_16x16x32_bf16 v[44:47], a[100:103], a[20:23], v[44:47]\n"
    "v_mfma_f32_16x16x32_bf16 v[44:47], a[108:111], a[44:47], v[44:47]\n"
    "v_mfma_f32_16x16x32_bf16 v[44:47], a[116:119], a[68:71], v[44:47]\n"
    "v_mfma_f32_16x16x32_bf16 v[44:47], a[124:127], a[92:95], v[44:47]\n"
    "s_barrier\n"
    "OQA%=:\n"
    "ds_read_b32 v120, v81\n"
    "ds_read_b128 a[12:15], v86 offset:3072\n"
    "ds_read_b128 a[36:39], v86 offset:9216\n"
    "ds_read_b128 a[60:63], v86 offset:15360\n"
    "ds_read_b128 a[84:87], v86 offset:21504\n"
    "ds_read_b128 a[8:11], v86 offset:2048\n"
    "ds_read_b128 a[32:35], v86 offset:8192\n"
    "ds_read_b128 a[56:59], v86 offset:14336\n"
    "ds_read_b128 a[80:83], v86 offset:20480\n"
    "s_waitcnt lgkmcnt(8)\n"
    "v_cmp_eq_u32 vcc, s25, v120\n"
    "s_cbranch_vccnz ROQA%=\n"
    "s_add_u32 s25, s25, 1\n"
    "s_mov_b32 s75, 0\n"
    "s_add_u32 s57, s92, 0x20000\n"
    "buffer_load_dword v92, off, s[80:83], s57 sc1\n"
    "ds_read_b128 a[0:3], v86\n"
    "ds_read_b128 a[24:27], v86 offset:6144\n"
    "ds_read_b128 a[48:51], v86 offset:12288\n"
    "ds_read_b128 a[72:75], v86 offset:18432\n"
    "ds_read_b128 a[4:7], v86 offset:1024\n"
    "ds_read_b128 a[28:31], v86 offset:7168\n"
    "s_waitcnt lgkmcnt(7)\n"
    "ds_read_b128 a[52:55], v86 offset:13312\n"
    "ds_read_b128 a[76:79], v86 offset:19456\n"
    "s_waitcnt vmcnt(8) lgkmcnt(0)\n"
    "s_barrier\n"
    "ds_read_b64_tr_b16 a[96:97], v84\n"
    "ds_read_b64_tr_b16 a[98:99], v84 offset:4096\n"
    "ds_read_b64_tr_b16 a[104:105], v84 offset:8192\n"
    "ds_read_b64_tr_b16 a[106:107], v84 offset:12288\n"
    "ds_read_b64_tr_b16 a[112:113], v84 offset:16384\n"
    "ds_read_b64_tr_b16 a[114:115], v84 offset:20480\n"
    "ds_read_b64_tr_b16 a[120:121], v84 offset:24576\n"
    "ds_read_b64_tr_b16 a[122:123], v84 offset:28672\n"
    "ds_read_b64_tr_b16 a[100:101], v85\n"
    "ds_read_b64_tr_b16 a[102:103], v85 offset:4096\n"
    "ds_read_b64_tr_b16 a[108:109], v85 offset:8192\n"
    "ds_read_b64_tr_b16 a[110:111], v85 offset:12288\n"
    "ds_read_b64_tr_b16 a[116:117], v85 offset:16384\n"
    "ds_read_b64_tr_b16 a[118:119], v85 offset:20480\n"
    "s_waitcnt lgkmcnt(12)\n"
    "v_mfma_f32_16x16x32_bf16 v[24:27], a[96:99], a[12:15], v[24:27]\n"
    "ds_read_b64_tr_b16 a[124:125], v85 offset:24576\n"
    "ds_read_b64_tr_b16 a[126:127], v85 offset:28672\n"
    "s_waitcnt lgkmcnt(12)\n"
    "v_mfma_f32_16x16x32_bf16 v[24:27], a[104:107], a[36:39], v[24:27]\n"
    "s_waitcnt lgkmcnt(10)\n"
    "v_mfma_f32_16x16x32_bf16 v[24:27], a[112:115], a[60:63], v[24:27]\n"
    "s_waitcnt lgkmcnt(8)\n"
    "v_mfma_f32_16x16x32_bf16 v[24:27], a[120:123], a[84:87], v[24:27]\n"
    "ds_read_b128 a[16:19], v86 offset:4096\n"
    "s_waitcnt lgkmcnt(7)\n"
    "v_mfma_f32_16x16x32_bf16 v[28:31], a[100:103], a[12:15], v[28:31]\n"
    "ds_read_b128 a[40:43], v86 offset:10240\n"
    "s_waitcnt lgkmcnt(6)\n"
    "v_mfma_f32_16x16x32_bf16 v[28:31], a[108:111], a[36:39], v[28:31]\n"
    "ds_read_b128 a[64:67], v86 offset:16384\n"
    "s_waitcnt lgkmcnt(5)\n"
    "v_mfma_f32_16x16x32_bf16 v[28:31], a[116:119], a[60:63], v[28:31]\n"
    "ds_read_b128 a[88:91], v86 offset:22528\n"
    "s_waitcnt lgkmcnt(4)\n"
    "v_mfma_f32_16x16x32_bf16 v[28:31], a[124:127], a[84:87], v[28:31]\n"
    "ds_read_b128 a[20:23], v86 offset:5120\n"
    "v_mfma_f32_16x16x32_bf16 v[16:19], a[96:99], a[8:11], v[16:19]\n"
    "v_xor_b32 v84, 0x8000, v84\n"
    "v_xor_b32 v85, 0x8000, v85\n"
    "ds_read_b128 a[44:47], v86 offset:11264\n"
    "v_mfma_f32_16x16x32_bf16 v[16:19], a[104:107], a[32:35], v[16:19]\n"
    "ds_read_b128 a[68:71], v86 offset:17408\n"
    "v_mfma_f32_16x16x32_bf16 v[16:19], a[112:115], a[56:59], v[16:19]\n"
    "ds_read_b128 a[92:95], v86 offset:23552\n"
    "v_mfma_f32_16x16x32_bf16 v[16:19], a[120:123], a[80:83], v[16:19]\n"
    "ds_read_b128 v[96:99], v88 offset:768\n"
    "ds_read_b128 v[100:103], v88 offset:1024\n"
    "v_mfma_f32_16x16x32_bf16 v[20:23], a[100:103], a[8:11], v[20:23]\n"
    "ds_read_b128 v[104:107], v88 offset:1280\n"
    "v_mfma_f32_16x16x32_bf16 v[20:23], a[108:111], a[32:35], v[20:23]\n"
    "v_mfma_f32_16x16x32_bf16 v[20:23], a[116:119], a[56:59], v[20:23]\n"
    "v_mfma_f32_16x16x32_bf16 v[20:23], a[124:127], a[80:83], v[20:23]\n"
    "s_waitcnt lgkmcnt(0)\n"
    "v_mfma_f32_16x16x32_bf16 v[32:35], a[96:99], a[16:19], v[32:35]\n"
    "v_add_f32 v96, v96, v97\n"
    "v_add_f32 v98, v98, v99\n"
    "v_add_f32 v96, v96, v98\n"
    "v_mfma_f32_16x16x32_bf16 v[32:35], a[104:107], a[40:43], v[32:35]\n"
    "v_rcp_f32 v98, v96\n"
    "v_log_f32 v97, v96\n"
    "s_nop 0\n"
    "v_mfma_f32_16x16x32_bf16 v[32:35], a[112:115], a[64:67], v[32:35]\n"
    "v_add_f32 v72, v97, v67\n"
    "v_add_f32 v100, v100, v101\n"
    "v_add_f32 v102, v102, v103\n"
    "v_mfma_f32_16x16x32_bf16 v[32:35], a[120:123], a[88:91], v[32:35]\n"
    "v_add_f32 v100, v100, v102\n"
    "v_rcp_f32 v102, v100\n"
    "v_mfma_f32_16x16x32_bf16 v[36:39], a[100:103], a[16:19], v[36:39]\n"
    "v_log_f32 v101, v100\n"
    "s_nop 0\n"
    "v_add_f32 v73, v101, v68\n"
    "v_mfma_f32_16x16x32_bf16 v[36:39], a[108:111], a[40:43], v[36:39]\n"
    "v_add_f32 v104, v104, v105\n"
    "v_add_f32 v106, v106, v107\n"
    "v_add_f32 v104, v104, v106\n"
    "v_mfma_f32_16x16x32_bf16 v[36:39], a[116:119], a[64:67], v[36:39]\n"
    "v_rcp_f32 v106, v104\n"
    "v_log_f32 v105, v104\n"
    "s_nop 0\n"
    "v_mfma_f32_16x16x32_bf16 v[36:39], a[124:127], a[88:91], v[36:39]\n"
    "v_add_f32 v74, v105, v69\n"
    "buffer_store_dwordx4 v[72:75], v87, s[80:83], s78 offen offset:3072 sc1\n"
    "v_mfma_f32_16x16x32_bf16 v[40:43], a[96:99], a[20:23], v[40:43]\n"
    "v_mul_f32 v48, v24, v98\n"
    "v_mul_f32 v49, v25, v98\n"
    "v_mul_f32 v50, v26, v98\n"
    "v_mfma_f32_16x16x32_bf16 v[40:43], a[104:107], a[44:47], v[40:43]\n"
    "v_mul_f32 v51, v27, v98\n"
    "v_mul_f32 v52, v28, v98\n"
    "v_mfma_f32_16x16x32_bf16 v[40:43], a[112:115], a[68:71], v[40:43]\n"
    "v_mul_f32 v53, v29, v98\n"
    "v_mul_f32 v54, v30, v98\n"
    "v_mfma_f32_16x16x32_bf16 v[40:43], a[120:123], a[92:95], v[40:43]\n"
    "v_mul_f32 v55, v31, v98\n"
    "v_cvt_pk_bf16_f32 v60, v48, v49\n"
    "v_mfma_f32_16x16x32_bf16 v[44:47], a[100:103], a[20:23], v[44:47]\n"
    "v_cvt_pk_bf16_f32 v61, v50, v51\n"
    "v_cvt_pk_bf16_f32 v62, v52, v53\n"
    "v_mfma_f32_16x16x32_bf16 v[44:47], a[108:111], a[44:47], v[44:47]\n"
    "v_cvt_pk_bf16_f32 v63, v54, v55\n"
    "buffer_store_dwordx4 v[60:63], v87, s[80:83], s78 offen sc1\n"
    "v_mfma_f32_16x16x32_bf16 v[44:47], a[116:119], a[68:71], v[44:47]\n"
    "v_mul_f32 v48, v32, v102\n"
    "v_mul_f32 v49, v33, v102\n"
    "v_mul_f32 v50, v34, v102\n"
    "v_mfma_f32_16x16x32_bf16 v[44:47], a[124:127], a[92:95], v[44:47]\n"
    "v_mul_f32 v51, v35, v102\n"
    "v_mul_f32 v52, v36, v102\n"
    "v_mfma_f32_16x16x32_bf16 v[0:3], a[96:99], a[0:3], v[0:3]\n"
    "v_mul_f32 v53, v37, v102\n"
    "v_mul_f32 v54, v38, v102\n"
    "v_mfma_f32_16x16x32_bf16 v[0:3], a[104:107], a[24:27], v[0:3]\n"
    "v_mul_f32 v55, v39, v102\n"
    "v_cvt_pk_bf16_f32 v56, v48, v49\n"
    "v_mfma_f32_16x16x32_bf16 v[0:3], a[112:115], a[48:51], v[0:3]\n"
    "v_cvt_pk_bf16_f32 v57, v50, v51\n"
    "v_cvt_pk_bf16_f32 v58, v52, v53\n"
    "v_mfma_f32_16x16x32_bf16 v[0:3], a[120:123], a[72:75], v[0:3]\n"
    "v_cvt_pk_bf16_f32 v59, v54, v55\n"
    "buffer_store_dwordx4 v[56:59], v87, s[80:83], s78 offen offset:1024 sc1\n"
    "v_mfma_f32_16x16x32_bf16 v[4:7], a[100:103], a[0:3], v[4:7]\n"
    "v_mul_f32 v48, v40, v106\n"
    "v_mul_f32 v49, v41, v106\n"
    "v_mul_f32 v50, v42, v106\n"
    "v_mfma_f32_16x16x32_bf16 v[4:7], a[108:111], a[24:27], v[4:7]\n"
    "v_mul_f32 v51, v43, v106\n"
    "v_mul_f32 v52, v44, v106\n"
    "v_mfma_f32_16x16x32_bf16 v[4:7], a[116:119], a[48:51], v[4:7]\n"
    "v_mul_f32 v53, v45, v106\n"
    "v_mul_f32 v54, v46, v106\n"
    "v_mfma_f32_16x16x32_bf16 v[4:7], a[124:127], a[72:75], v[4:7]\n"
    "v_mul_f32 v55, v47, v106\n"
    "v_cvt_pk_bf16_f32 v60, v48, v49\n"
    "v_mfma_f32_16x16x32_bf16 v[8:11], a[96:99], a[4:7], v[8:11]\n"
    "v_cvt_pk_bf16_f32 v61, v50, v51\n"
    "v_cvt_pk_bf16_f32 v62, v52, v53\n"
    "v_mfma_f32_16x16x32_bf16 v[8:11], a[104:107], a[28:31], v[8:11]\n"
    "v_cvt_pk_bf16_f32 v63, v54, v55\n"
    "buffer_store_dwordx4 v[60:63], v87, s[80:83], s78 offen offset:2048 sc1\n"
    "v_mfma_f32_16x16x32_bf16 v[8:11], a[112:115], a[52:55], v[8:11]\n"
    "v_mfma_f32_16x16x32_bf16 v[8:11], a[120:123], a[76:79], v[8:11]\n"
    "v_mfma_f32_16x16x32_bf16 v[12:15], a[100:103], a[4:7], v[12:15]\n"
    "v_mfma_f32_16x16x32_bf16 v[12:15], a[108:111], a[28:31], v[12:15]\n"
    "v_mfma_f32_16x16x32_bf16 v[12:15], a[116:119], a[52:55], v[12:15]\n"
    "v_mfma_f32_16x16x32_bf16 v[12:15], a[124:127], a[76:79], v[12:15]\n"
    "s_barrier\n"
    "OPA%=:\n"
    "ds_read_b32 v120, v81\n"
    "ds_read_b128 a[0:3], v86\n"
    "ds_read_b128 a[24:27], v86 offset:6144\n"
    "ds_read_b128 a[48:51], v86 offset:12288\n"
    "ds_read_b128 a[72:75], v86 offset:18432\n"
    "ds_read_b128 a[4:7], v86 offset:1024\n"
    "ds_read_b128 a[28:31], v86 offset:7168\n"
    "ds_read_b128 a[52:55], v86 offset:13312\n"
    "ds_read_b128 a[76:79], v86 offset:19456\n"
    "s_waitcnt lgkmcnt(8)\n"
    "v_cmp_eq_u32 vcc, s25, v120\n"
    "s_cbranch_vccnz ROPA%=\n"
    "s_add_u32 s25, s25, 1\n"
    "s_mov_b32 s75, 0\n"
    "s_mov_b32 s22, s89\n"
    "s_mov_b32 m0, s24\n"
    "s_add_u32 s54, s22, 0x1000\n"
    "buffer_load_dwordx4 v80, s[40:43], s22 offen lds\n"
    "buffer_load_dwordx4 v80, s[40:43], s22 offen offset:2048 lds\n"
    "s_add_u32 m0, s24, 896\n"
    "s_nop 0\n"
    "buffer_load_dwordx4 v80, s[40:43], s22 offen offset:128 lds\n"
    "buffer_load_dwordx4 v80, s[40:43], s22 offen offset:2176 lds\n"
    "ds_read_b128 a[8:11], v86 offset:2048\n"
    "ds_read_b128 a[32:35], v86 offset:8192\n"
    "ds_read_b128 a[56:59], v86 offset:14336\n"
    "ds_read_b128 a[80:83], v86 offset:20480\n"
    "s_add_u32 m0, s24, 0x1000\n"
    "s_add_u32 s22, s22, 0x8000\n"
    "buffer_load_dwordx4 v80, s[40:43], s54 offen lds\n"
    "buffer_load_dwordx4 v80, s[40:43], s54 offen offset:2048 lds\n"
    "s_add_u32 m0, s24, 4992\n"
    "s_xor_b32 s24, s24, 0x8000\n"
    "buffer_load_dwordx4 v80, s[40:43], s54 offen offset:128 lds\n"
    "buffer_load_dwordx4 v80, s[40:43], s54 offen offset:2176 lds\n"
    "s_waitcnt vmcnt(8)\n"
    "v_readfirstlane_b32 s28, v92\n"
    "buffer_store_dword v92, v90, s[80:83], s92 offen sc1\n"
    "s_waitcnt lgkmcnt(0)\n"
    "s_barrier\n"
    "ds_read_b64_tr_b16 a[96:97], v84\n"
    "ds_read_b64_tr_b16 a[98:99], v84 offset:4096\n"
    "ds_read_b64_tr_b16 a[104:105], v84 offset:8192\n"
    "ds_read_b64_tr_b16 a[106:107], v84 offset:12288\n"
    "ds_read_b64_tr_b16 a[112:113], v84 offset:16384\n"
    "ds_read_b64_tr_b16 a[114:115], v84 offset:20480\n"
    "ds_read_b64_tr_b16 a[120:121], v84 offset:24576\n"
    "ds_read_b64_tr_b16 a[122:123], v84 offset:28672\n"
    "ds_read_b64_tr_b16 a[100:101], v85\n"
    "ds_read_b64_tr_b16 a[102:103], v85 offset:4096\n"
    "ds_read_b64_tr_b16 a[108:109], v85 offset:8192\n"
    "ds_read_b64_tr_b16 a[110:111], v85 offset:12288\n"
    "ds_read_b64_tr_b16 a[116:117], v85 offset:16384\n"
    "ds_read_b64_tr_b16 a[118:119], v85 offset:20480\n"
    "s_waitcnt lgkmcnt(12)\n"
    "v_mfma_f32_16x16x32_bf16 v[0:3], a[96:99], a[0:3], v[0:3]\n"
    "ds_read_b64_tr_b16 a[124:125], v85 offset:24576\n"
    "ds_read_b64_tr_b16 a[126:127], v85 offset:28672\n"
    "s_waitcnt lgkmcnt(12)\n"
    "v_mfma_f32_16x16x32_bf16 v[0:3], a[104:107], a[24:27], v[0:3]\n"
    "s_waitcnt lgkmcnt(10)\n"
    "v_mfma_f32_16x16x32_bf16 v[0:3], a[112:115], a[48:51], v[0:3]\n"
    "s_waitcnt lgkmcnt(8)\n"
    "v_mfma_f32_16x16x32_bf16 v[0:3], a[120:123], a[72:75], v[0:3]\n"
    "s_waitcnt lgkmcnt(6)\n"
    "v_mfma_f32_16x16x32_bf16 v[4:7], a[100:103], a[0:3], v[4:7]\n"
    "s_waitcnt lgkmcnt(4)\n"
    "v_mfma_f32_16x16x32_bf16 v[4:7], a[108:111], a[24:27], v[4:7]\n"
    "s_waitcnt lgkmcnt(2)\n"
    "v_mfma_f32_16x16x32_bf16 v[4:7], a[116:119], a[48:51], v[4:7]\n"
    "s_waitcnt lgkmcnt(0)\n"
    "v_mfma_f32_16x16x32_bf16 v[4:7], a[124:127], a[72:75], v[4:7]\n"
    "v_mfma_f32_16x16x32_bf16 v[8:11], a[96:99], a[4:7], v[8:11]\n"
    "v_xor_b32 v84, 0x8000, v84\n"
    "v_xor_b32 v85, 0x8000, v85\n"
    "buffer_load_dword v91, off, s[80:83], s91 sc1\n"
    "v_mfma_f32_16x16x32_bf16 v[8:11], a[104:107], a[28:31], v[8:11]\n"
    "v_mfma_f32_16x16x32_bf16 v[8:11], a[112:115], a[52:55], v[8:11]\n"
    "v_mfma_f32_16x16x32_bf16 v[8:11], a[120:123], a[76:79], v[8:11]\n"
    "v_mfma_f32_16x16x32_bf16 v[12:15], a[100:103], a[4:7], v[12:15]\n"
    "v_mfma_f32_16x16x32_bf16 v[12:15], a[108:111], a[28:31], v[12:15]\n"
    "v_mfma_f32_16x16x32_bf16 v[12:15], a[116:119], a[52:55], v[12:15]\n"
    "v_mfma_f32_16x16x32_bf16 v[12:15], a[124:127], a[76:79], v[12:15]\n"
    "v_mfma_f32_16x16x32_bf16 v[16:19], a[96:99], a[8:11], v[16:19]\n"
    "v_mfma_f32_16x16x32_bf16 v[16:19], a[104:107], a[32:35], v[16:19]\n"
    "v_mfma_f32_16x16x32_bf16 v[16:19], a[112:115], a[56:59], v[16:19]\n"
    "v_mfma_f32_16x16x32_bf16 v[16:19], a[120:123], a[80:83], v[16:19]\n"
    "v_mfma_f32_16x16x32_bf16 v[20:23], a[100:103], a[8:11], v[20:23]\n"
    "v_mfma_f32_16x16x32_bf16 v[20:23], a[108:111], a[32:35], v[20:23]\n"
    "v_mfma_f32_16x16x32_bf16 v[20:23], a[116:119], a[56:59], v[20:23]\n"
    "v_mfma_f32_16x16x32_bf16 v[20:23], a[124:127], a[80:83], v[20:23]\n"
    "s_waitcnt vmcnt(0)\n"
    "v_cmp_ne_u32 vcc, s27, v91\n"
    "s_cbranch_vccnz POLL2%=\n"
    "buffer_load_dwordx4 v[108:111], v87, s[80:83], s79 offen offset:3072 sc1\n"
    "buffer_load_dwordx4 v[96:99], v87, s[80:83], s79 offen sc1\n"
    "buffer_load_dwordx4 v[100:103], v87, s[80:83], s79 offen offset:1024 sc1\n"
    "buffer_load_dwordx4 v[104:107], v87, s[80:83], s79 offen offset:2048 sc1\n"
    "s_add_u32 s57, s92, 0x20000\n"
    "buffer_load_dword v92, off, s[80:83], s57 sc1\n"
    "s_mov_b32 s90, 1\n"
    "POLL3%=:\n"
    "s_barrier\n"
    "OFA%=:\n"
    "ds_read_b32 v120, v81\n"
    "ds_read_b64_tr_b16 a[96:97], v84\n"
    "ds_read_b64_tr_b16 a[98:99], v84 offset:4096\n"
    "ds_read_b128 a[0:3], v86\n"
    "ds_read_b64_tr_b16 a[104:105], v84 offset:8192\n"
    "ds_read_b64_tr_b16 a[106:107], v84 offset:12288\n"
    "ds_read_b128 a[24:27], v86 offset:6144\n"
    "ds_read_b64_tr_b16 a[112:113], v84 offset:16384\n"
    "ds_read_b64_tr_b16 a[114:115], v84 offset:20480\n"
    "ds_read_b128 a[48:51], v86 offset:12288\n"
    "ds_read_b64_tr_b16 a[120:121], v84 offset:24576\n"
    "ds_read_b64_tr_b16 a[122:123], v84 offset:28672\n"
    "ds_read_b128 a[72:75], v86 offset:18432\n"
    "s_waitcnt lgkmcnt(12)\n"
    "v_cmp_eq_u32 vcc, s25, v120\n"
    "s_cbranch_vccnz ROFA%=\n"
    "s_add_u32 s25, s25, 1\n"
    "s_mov_b32 s75, 0\n"
    "s_waitcnt lgkmcnt(9)\n"
    "v_mfma_f32_16x16x32_bf16 v[0:3], a[96:99], a[0:3], v[0:3]\n"
    "ds_read_b64_tr_b16 a[100:101], v85\n"
    "ds_read_b64_tr_b16 a[102:103], v85 offset:4096\n"
    "s_waitcnt lgkmcnt(8)\n"
    "v_mfma_f32_16x16x32_bf16 v[0:3], a[104:107], a[24:27], v[0:3]\n"
    "ds_read_b64_tr_b16 a[108:109], v85 offset:8192\n"
    "ds_read_b64_tr_b16 a[110:111], v85 offset:12288\n"
    "s_waitcnt lgkmcnt(7)\n"
    "v_mfma_f32_16x16x32_bf16 v[0:3], a[112:115], a[48:51], v[0:3]\n"
    "ds_read_b64_tr_b16 a[116:117], v85 offset:16384\n"
    "ds_read_b64_tr_b16 a[118:119], v85 offset:20480\n"
    "s_cmp_eq_u32 s90, 0\n"
    "s_cbranch_scc1 CHK2%=\n"
    "CHKX%=:\n"
    "s_waitcnt lgkmcnt(6)\n"
    "v_mfma_f32_16x16x32_bf16 v[0:3], a[120:123], a[72:75], v[0:3]\n"
    "ds_read_b64_tr_b16 a[124:125], v85 offset:24576\n"
    "ds_read_b64_tr_b16 a[126:127], v85 offset:28672\n"
    "s_mul_i32 s59, s20, 5120\n"
    "v_and_b32 v126, 15, v82\n"
    "v_mul_u32_u24 v126, 80, v126\n"
    "s_waitcnt lgkmcnt(6)\n"
    "v_mfma_f32_16x16x32_bf16 v[4:7], a[100:103], a[0:3], v[4:7]\n"
    "ds_read_b128 a[4:7], v86 offset:1024\n"
    "ds_read_b128 a[28:31], v86 offset:7168\n"
    "v_lshrrev_b32 v120, 4, v82\n"
    "v_lshl_add_u32 v126, v120, 3, v126\n"
    "s_waitcnt lgkmcnt(6)\n"
    "v_mfma_f32_16x16x32_bf16 v[4:7], a[108:111], a[24:27], v[4:7]\n"
    "ds_read_b128 a[52:55], v86 offset:13312\n"
    "v_add_u32 v126, s59, v126\n"
    "v_lshrrev_b32 v120, 2, v82\n"
    "s_waitcnt lgkmcnt(5)\n"
    "v_mfma_f32_16x16x32_bf16 v[4:7], a[116:119], a[48:51], v[4:7]\n"
    "ds_read_b128 a[76:79], v86 offset:19456\n"
    "v_and_b32 v79, 3, v82\n"
    "v_lshlrev_b32 v79, 4, v79\n"
    "s_waitcnt lgkmcnt(4)\n"
    "v_mfma_f32_16x16x32_bf16 v[4:7], a[124:127], a[72:75], v[4:7]\n"
    "ds_read_b128 v[48:51], v88\n"
    "v_mul_u32_u24 v78, 80, v120\n"
    "v_add_u32 v78, v78, v79\n"
    "s_waitcnt lgkmcnt(1)\n"
    "v_mfma_f32_16x16x32_bf16 v[8:11], a[96:99], a[4:7], v[8:11]\n"
    "ds_read_b128 v[52:55], v88 offset:256\n"
    "v_add_u32 v78, s59, v78\n"
    "v_lshl_add_u32 v79, v120, 8, v79\n"
    "v_mfma_f32_16x16x32_bf16 v[8:11], a[104:107], a[28:31], v[8:11]\n"
    "ds_read_b128 v[56:59], v88 offset:512\n"
    "ds_read_b128 a[8:11], v86 offset:2048\n"
    "ds_read_b128 a[32:35], v86 offset:8192\n"
    "v_mfma_f32_16x16x32_bf16 v[8:11], a[112:115], a[52:55], v[8:11]\n"
    "ds_read_b128 a[56:59], v86 offset:14336\n"
    "v_mfma_f32_16x16x32_bf16 v[8:11], a[120:123], a[76:79], v[8:11]\n"
    "ds_read_b128 a[80:83], v86 offset:20480\n"
    "v_mfma_f32_16x16x32_bf16 v[12:15], a[100:103], a[4:7], v[12:15]\n"
    "s_waitcnt lgkmcnt(4)\n"
    "v_add_f32 v48, v48, v49\n"
    "v_add_f32 v50, v50, v51\n"
    "v_mfma_f32_16x16x32_bf16 v[12:15], a[108:111], a[28:31], v[12:15]\n"
    "v_add_f32 v48, v48, v50\n"
    "v_add_f32 v52, v52, v53\n"
    "v_mfma_f32_16x16x32_bf16 v[12:15], a[116:119], a[52:55], v[12:15]\n"
    "v_add_f32 v54, v54, v55\n"
    "v_add_f32 v52, v52, v54\n"
    "v_add_f32 v56, v56, v57\n"
    "v_mfma_f32_16x16x32_bf16 v[12:15], a[124:127], a[76:79], v[12:15]\n"
    "v_add_f32 v58, v58, v59\n"
    "v_add_f32 v56, v56, v58\n"
    "s_waitcnt lgkmcnt(0)\n"
    "v_mfma_f32_16x16x32_bf16 v[16:19], a[96:99], a[8:11], v[16:19]\n"
    "s_waitcnt vmcnt(0)\n"
    "v_cmp_ne_u32 vcc, s28, v92\n"
    "s_cbranch_vccnz ECHO%=\n"
    "ECHOK%=:\n"
    "s_lshl_b32 s57, s20, 6\n"
    "s_add_u32 s57, s57, s94\n"
    "v_max_f32 v76, v64, v108\n"
    "v_sub_f32 v70, v64, v76\n"
    "v_mfma_f32_16x16x32_bf16 v[16:19], a[104:107], a[32:35], v[16:19]\n"
    "v_sub_f32 v73, v108, v76\n"
    "v_exp_f32 v70, v70\n"
    "v_mfma_f32_16x16x32_bf16 v[16:19], a[112:115], a[56:59], v[16:19]\n"
    "v_exp_f32 v73, v73\n"
    "v_lshlrev_b32 v24, 16, v96\n"
    "v_mfma_f32_16x16x32_bf16 v[16:19], a[120:123], a[80:83], v[16:19]\n"
    "v_fma_f32 v48, v48, v70, v73\n"
    "v_rcp_f32 v48, v48\n"
    "v_mfma_f32_16x16x32_bf16 v[20:23], a[100:103], a[8:11], v[20:23]\n"
    "v_and_b32 v25, 0xffff0000, v96\n"
    "v_mul_f32 v73, v73, v48\n"
    "v_mfma_f32_16x16x32_bf16 v[20:23], a[108:111], a[32:35], v[20:23]\n"
    "v_mul_f32 v70, v70, v48\n"
    "v_lshlrev_b32 v26, 16, v97\n"
    "v_mfma_f32_16x16x32_bf16 v[20:23], a[116:119], a[56:59], v[20:23]\n"
    "v_and_b32 v27, 0xffff0000, v97\n"
    "v_lshlrev_b32 v28, 16, v98\n"
    "v_mfma_f32_16x16x32_bf16 v[20:23], a[124:127], a[80:83], v[20:23]\n"
    "v_and_b32 v29, 0xffff0000, v98\n"
    "v_lshlrev_b32 v30, 16, v99\n"
    "v_and_b32 v31, 0xffff0000, v99\n"
    "v_mul_f32 v24, v24, v73\n"
    "v_mul_f32 v25, v25, v73\n"
    "v_mul_f32 v26, v26, v73\n"
    "v_mul_f32 v27, v27, v73\n"
    "v_mul_f32 v28, v28, v73\n"
    "v_mul_f32 v29, v29, v73\n"
    "v_mul_f32 v30, v30, v73\n"
    "v_mul_f32 v31, v31, v73\n"
    "v_max_f32 v76, v65, v109\n"
    "v_sub_f32 v71, v65, v76\n"
    "v_sub_f32 v74, v109, v76\n"
    "v_exp_f32 v71, v71\n"
    "v_exp_f32 v74, v74\n"
    "v_lshlrev_b32 v32, 16, v100\n"
    "v_fma_f32 v52, v52, v71, v74\n"
    "v_rcp_f32 v52, v52\n"
    "v_and_b32 v33, 0xffff0000, v100\n"
    "v_mul_f32 v74, v74, v52\n"
    "v_mul_f32 v71, v71, v52\n"
    "v_lshlrev_b32 v34, 16, v101\n"
    "v_and_b32 v35, 0xffff0000, v101\n"
    "v_lshlrev_b32 v36, 16, v102\n"
    "v_and_b32 v37, 0xffff0000, v102\n"
    "v_lshlrev_b32 v38, 16, v103\n"
    "v_and_b32 v39, 0xffff0000, v103\n"
    "v_mul_f32 v32, v32, v74\n"
    "v_mul_f32 v33, v33, v74\n"
    "v_mul_f32 v34, v34, v74\n"
    "v_mul_f32 v35, v35, v74\n"
    "v_mul_f32 v36, v36, v74\n"
    "v_mul_f32 v37, v37, v74\n"
    "v_mul_f32 v38, v38, v74\n"
    "v_mul_f32 v39, v39, v74\n"
    "v_max_f32 v76, v66, v110\n"
    "v_sub_f32 v72, v66, v76\n"
    "v_sub_f32 v75, v110, v76\n"
    "v_exp_f32 v72, v72\n"
    "v_exp_f32 v75, v75\n"
    "v_lshlrev_b32 v40, 16, v104\n"
    "v_fma_f32 v56, v56, v72, v75\n"
    "v_rcp_f32 v56, v56\n"
    "v_and_b32 v41, 0xffff0000, v104\n"
    "v_mul_f32 v75, v75, v56\n"
    "v_mul_f32 v72, v72, v56\n"
    "v_lshlrev_b32 v42, 16, v105\n"
    "v_and_b32 v43, 0xffff0000, v105\n"
    "v_lshlrev_b32 v44, 16, v106\n"
    "v_and_b32 v45, 0xffff0000, v106\n"
    "v_lshlrev_b32 v46, 16, v107\n"
    "v_and_b32 v47, 0xffff0000, v107\n"
    "v_mul_f32 v40, v40, v75\n"
    "v_mul_f32 v41, v41, v75\n"
    "v_mul_f32 v42, v42, v75\n"
    "v_mul_f32 v43, v43, v75\n"
    "v_mul_f32 v44, v44, v75\n"
    "v_mul_f32 v45, v45, v75\n"
    "v_mul_f32 v46, v46, v75\n"
    "v_mul_f32 v47, v47, v75\n"
    "v_fmac_f32 v24, v0, v70\n"
    "v_fmac_f32 v25, v1, v70\n"
    "v_fmac_f32 v26, v2, v70\n"
    "v_fmac_f32 v27, v3, v70\n"
    "v_fmac_f32 v28, v4, v70\n"
    "v_fmac_f32 v29, v5, v70\n"
    "v_fmac_f32 v30, v6, v70\n"
    "v_fmac_f32 v31, v7, v70\n"
    "v_cvt_pk_bf16_f32 v24, v24, v25\n"
    "v_cvt_pk_bf16_f32 v25, v26, v27\n"
    "v_cvt_pk_bf16_f32 v26, v28, v29\n"
    "v_cvt_pk_bf16_f32 v27, v30, v31\n"
    "ds_write_b64 v126, v[24:25]\n"
    "ds_write_b64 v126, v[26:27] offset:32\n"
    "ds_read_b128 v[56:59], v78\n"
    "v_fmac_f32 v32, v8, v71\n"
    "v_fmac_f32 v33, v9, v71\n"
    "v_fmac_f32 v34, v10, v71\n"
    "v_fmac_f32 v35, v11, v71\n"
    "v_fmac_f32 v36, v12, v71\n"
    "v_fmac_f32 v37, v13, v71\n"
    "v_fmac_f32 v38, v14, v71\n"
    "v_fmac_f32 v39, v15, v71\n"
    "v_cvt_pk_bf16_f32 v32, v32, v33\n"
    "v_cvt_pk_bf16_f32 v33, v34, v35\n"
    "v_cvt_pk_bf16_f32 v34, v36, v37\n"
    "v_cvt_pk_bf16_f32 v35, v38, v39\n"
    "ds_write_b64 v126, v[32:33] offset:1280\n"
    "ds_write_b64 v126, v[34:35] offset:1312\n"
    "ds_read_b128 v[116:119], v78 offset:1280\n"
    "s_waitcnt lgkmcnt(3)\n"
    "buffer_store_dwordx4 v[56:59], v79, s[48:51], s57 offen sc1\n"
    "s_add_u32 s57, s57, 0x1000\n"
    "v_fmac_f32 v40, v16, v72\n"
    "v_fmac_f32 v41, v17, v72\n"
    "v_fmac_f32 v42, v18, v72\n"
    "v_fmac_f32 v43, v19, v72\n"
    "v_fmac_f32 v44, v20, v72\n"
    "v_fmac_f32 v45, v21, v72\n"
    "v_fmac_f32 v46, v22, v72\n"
    "v_fmac_f32 v47, v23, v72\n"
    "v_cvt_pk_bf16_f32 v40, v40, v41\n"
    "v_cvt_pk_bf16_f32 v41, v42, v43\n"
    "v_cvt_pk_bf16_f32 v42, v44, v45\n"
    "v_cvt_pk_bf16_f32 v43, v46, v47\n"
    "ds_write_b64 v126, v[40:41] offset:2560\n"
    "ds_write_b64 v126, v[42:43] offset:2592\n"
    "ds_read_b128 v[122:125], v78 offset:2560\n"
    "s_waitcnt lgkmcnt(3)\n"
    "buffer_store_dwordx4 v[116:119], v79, s[48:51], s57 offen sc1\n"
    "s_add_u32 s57, s57, 0x1000\n"
    "s_waitcnt lgkmcnt(0)\n"
    "buffer_store_dwordx4 v[122:125], v79, s[48:51], s57 offen sc1\n"
    "s_add_u32 s57, s57, 0x1000\n"
    "s_endpgm\n"
    "KEND%=:\n"
    "RSL3A%=:\n"
    "s_nop 4\n"
    "s_call_b64 s[30:31], SREDO%=\n"
    "s_branch SL3A%=\n"
    "LOW%=:\n"
    "s_nop 4\n"
    "v_or3_b32 v121, v0, v1, v2\n"
    "v_or_b32 v121, v121, v3\n"
    "v_and_b32 v121, 0x60006000, v121\n"
    "v_cmp_eq_u32 s[60:61], 0, v121\n"
    "s_or_b64 s[58:59], s[58:59], s[60:61]\n"
    "v_or3_b32 v121, v8, v9, v10\n"
    "v_or_b32 v121, v121, v11\n"
    "v_and_b32 v121, 0x60006000, v121\n"
    "v_cmp_eq_u32 s[60:61], 0, v121\n"
    "s_or_b64 s[58:59], s[58:59], s[60:61]\n"
    "v_or3_b32 v121, v16, v17, v18\n"
    "v_or_b32 v121, v121, v19\n"
    "v_and_b32 v121, 0x60006000, v121\n"
    "v_cmp_eq_u32 s[60:61], 0, v121\n"
    "s_or_b64 s[58:59], s[58:59], s[60:61]\n"
    "v_or3_b32 v121, v24, v25, v26\n"
    "v_or_b32 v121, v121, v27\n"
    "v_and_b32 v121, 0x60006000, v121\n"
    "v_cmp_eq_u32 s[60:61], 0, v121\n"
    "s_or_b64 s[58:59], s[58:59], s[60:61]\n"
    "v_mov_b32 v122, 0xc27c0000\n"
    "v_max3_f32 v121, v32, v33, v34\n"
    "v_max3_f32 v121, v121, v35, v36\n"
    "v_max3_f32 v121, v121, v37, v38\n"
    "v_max_f32 v121, v121, v39\n"
    "v_cmp_gt_f32 s[60:61], v122, v121\n"
    "s_or_b64 s[58:59], s[58:59], s[60:61]\n"
    "v_max3_f32 v121, v40, v41, v42\n"
    "v_max3_f32 v121, v121, v43, v44\n"
    "v_max3_f32 v121, v121, v45, v46\n"
    "v_max_f32 v121, v121, v47\n"
    "v_cmp_gt_f32 s[60:61], v122, v121\n"
    "s_or_b64 s[58:59], s[58:59], s[60:61]\n"
    "s_branch LOX%=\n"
    "PSL3%=:\n"
    "s_nop 4\n"
    "v_mov_b32 v121, s25\n"
    "ds_write_b32 v81, v121\n"
    "s_branch QSL3%=\n"
    "RSMA%=:\n"
    "s_nop 4\n"
    "s_call_b64 s[30:31], SREDO%=\n"
    "s_branch SMA%=\n"
    "PSM%=:\n"
    "s_nop 4\n"
    "v_mov_b32 v121, s25\n"
    "ds_write_b32 v81, v121\n"
    "s_branch QSM%=\n"
    "RSEA%=:\n"
    "s_nop 4\n"
    "s_call_b64 s[30:31], SREDO%=\n"
    "s_branch SEA%=\n"
    "PSE%=:\n"
    "s_nop 4\n"
    "v_mov_b32 v121, s25\n"
    "ds_write_b32 v81, v121\n"
    "s_branch QSE%=\n"
    "RSENDA%=:\n"
    "s_nop 4\n"
    "s_call_b64 s[30:31], SREDO%=\n"
    "s_branch SENDA%=\n"
    "ROLA%=:\n"
    "s_nop 4\n"
    "s_call_b64 s[30:31], OREDO%=\n"
    "s_branch OLA%=\n"
    "OW0%=:\n"
    "s_nop 4\n"
    "s_waitcnt vmcnt(8)\n"
    "s_branch OW1%=\n"
    "PE0%=:\n"
    "s_nop 4\n"
    "s_sleep 8\n"
    "s_branch PE1%=\n"
    "RO5A%=:\n"
    "s_nop 4\n"
    "s_call_b64 s[30:31], OREDO%=\n"
    "s_branch O5A%=\n"
    "ROQA%=:\n"
    "s_nop 4\n"
    "s_call_b64 s[30:31], OREDO%=\n"
    "s_branch OQA%=\n"
    "ROPA%=:\n"
    "s_nop 4\n"
    "s_call_b64 s[30:31], OREDO%=\n"
    "s_branch OPA%=\n"
    "POLL2%=:\n"
    "s_nop 4\n"
    "buffer_load_dword v91, off, s[80:83], s91 sc1\n"
    "s_branch POLL3%=\n"
    "ROFA%=:\n"
    "s_nop 4\n"
    "s_call_b64 s[30:31], OREDO%=\n"
    "s_branch OFA%=\n"
    "ECHO%=:\n"
    "s_nop 4\n"
    "buffer_store_dword v92, v90, s[80:83], s92 offen sc1\n"
    "s_branch ECHOK%=\n"
    "SREDO%=:\n"
    "s_nop 15\n"
    "v_and_b32 v121, 15, v82\n"
    "v_lshlrev_b32 v121, 8, v121\n"
    "v_and_or_b32 v121, v82, 48, v121\n"
    "s_add_u32 s56, s29, 0x1000\n"
    "buffer_load_dwordx4 a[96:99], v121, s[36:39], s29 offen\n"
    "buffer_load_dwordx4 a[100:103], v121, s[36:39], s29 offen offset:64\n"
    "buffer_load_dwordx4 a[104:107], v121, s[36:39], s29 offen offset:128\n"
    "buffer_load_dwordx4 a[108:111], v121, s[36:39], s29 offen offset:192\n"
    "buffer_load_dwordx4 a[112:115], v121, s[36:39], s56 offen\n"
    "buffer_load_dwordx4 a[116:119], v121, s[36:39], s56 offen offset:64\n"
    "buffer_load_dwordx4 a[120:123], v121, s[36:39], s56 offen offset:128\n"
    "buffer_load_dwordx4 a[124:127], v121, s[36:39], s56 offen offset:192\n"
    "s_cmp_eq_u32 s92, 2\n"
    "s_cbranch_scc1 SRN%=\n"
    "v_mov_b32 v96, v76\n"
    "v_mov_b32 v100, v77\n"
    "v_mov_b32 v104, v78\n"
    "s_cmp_eq_u32 s92, 1\n"
    "s_cbranch_scc1 SRN%=\n"
    "v_mov_b32 v108, v79\n"
    "SRN%=:\n"
    "s_waitcnt vmcnt(0) lgkmcnt(0)\n"
    "v_mfma_f32_16x16x32_bf16 v[0:3], a[96:99], a[0:3], v[48:51]\n"
    "v_mfma_f32_16x16x32_bf16 v[0:3], a[100:103], a[4:7], v[0:3]\n"
    "v_mfma_f32_16x16x32_bf16 v[0:3], a[104:107], a[8:11], v[0:3]\n"
    "v_mfma_f32_16x16x32_bf16 v[0:3], a[108:111], a[12:15], v[0:3]\n"
    "v_mfma_f32_16x16x32_bf16 v[4:7], a[112:115], a[0:3], v[48:51]\n"
    "v_mfma_f32_16x16x32_bf16 v[4:7], a[116:119], a[4:7], v[4:7]\n"
    "v_mfma_f32_16x16x32_bf16 v[4:7], a[120:123], a[8:11], v[4:7]\n"
    "v_mfma_f32_16x16x32_bf16 v[4:7], a[124:127], a[12:15], v[4:7]\n"
    "v_mfma_f32_16x16x32_bf16 v[8:11], a[96:99], a[16:19], v[52:55]\n"
    "v_mfma_f32_16x16x32_bf16 v[8:11], a[100:103], a[20:23], v[8:11]\n"
    "v_mfma_f32_16x16x32_bf16 v[8:11], a[104:107], a[24:27], v[8:11]\n"
    "v_mfma_f32_16x16x32_bf16 v[8:11], a[108:111], a[28:31], v[8:11]\n"
    "v_mfma_f32_16x16x32_bf16 v[12:15], a[112:115], a[16:19], v[52:55]\n"
    "v_mfma_f32_16x16x32_bf16 v[12:15], a[116:119], a[20:23], v[12:15]\n"
    "v_mfma_f32_16x16x32_bf16 v[12:15], a[120:123], a[24:27], v[12:15]\n"
    "v_mfma_f32_16x16x32_bf16 v[12:15], a[124:127], a[28:31], v[12:15]\n"
    "v_mfma_f32_16x16x32_bf16 v[16:19], a[96:99], a[32:35], v[56:59]\n"
    "v_mfma_f32_16x16x32_bf16 v[16:19], a[100:103], a[36:39], v[16:19]\n"
    "v_mfma_f32_16x16x32_bf16 v[16:19], a[104:107], a[40:43], v[16:19]\n"
    "v_mfma_f32_16x16x32_bf16 v[16:19], a[108:111], a[44:47], v[16:19]\n"
    "v_mfma_f32_16x16x32_bf16 v[20:23], a[112:115], a[32:35], v[56:59]\n"
    "v_mfma_f32_16x16x32_bf16 v[20:23], a[116:119], a[36:39], v[20:23]\n"
    "v_mfma_f32_16x16x32_bf16 v[20:23], a[120:123], a[40:43], v[20:23]\n"
    "v_mfma_f32_16x16x32_bf16 v[20:23], a[124:127], a[44:47], v[20:23]\n"
    "v_mfma_f32_16x16x32_bf16 v[24:27], a[96:99], a[48:51], v[60:63]\n"
    "v_mfma_f32_16x16x32_bf16 v[24:27], a[100:103], a[52:55], v[24:27]\n"
    "v_mfma_f32_16x16x32_bf16 v[24:27], a[104:107], a[56:59], v[24:27]\n"
    "v_mfma_f32_16x16x32_bf16 v[24:27], a[108:111], a[60:63], v[24:27]\n"
    "v_mfma_f32_16x16x32_bf16 v[28:31], a[112:115], a[48:51], v[60:63]\n"
    "v_mfma_f32_16x16x32_bf16 v[28:31], a[116:119], a[52:55], v[28:31]\n"
    "v_mfma_f32_16x16x32_bf16 v[28:31], a[120:123], a[56:59], v[28:31]\n"
    "v_mfma_f32_16x16x32_bf16 v[28:31], a[124:127], a[60:63], v[28:31]\n"
    "v_mfma_f32_16x16x32_bf16 v[32:35], a[96:99], a[64:67], v[64:67]\n"
    "v_mfma_f32_16x16x32_bf16 v[32:35], a[100:103], a[68:71], v[32:35]\n"
    "v_mfma_f32_16x16x32_bf16 v[32:35], a[104:107], a[72:75], v[32:35]\n"
    "v_mfma_f32_16x16x32_bf16 v[32:35], a[108:111], a[76:79], v[32:35]\n"
    "v_mfma_f32_16x16x32_bf16 v[36:39], a[112:115], a[64:67], v[64:67]\n"
    "v_mfma_f32_16x16x32_bf16 v[36:39], a[116:119], a[68:71], v[36:39]\n"
    "v_mfma_f32_16x16x32_bf16 v[36:39], a[120:123], a[72:75], v[36:39]\n"
    "v_mfma_f32_16x16x32_bf16 v[36:39], a[124:127], a[76:79], v[36:39]\n"
    "v_mfma_f32_16x16x32_bf16 v[40:43], a[96:99], a[80:83], v[68:71]\n"
    "v_mfma_f32_16x16x32_bf16 v[40:43], a[100:103], a[84:87], v[40:43]\n"
    "v_mfma_f32_16x16x32_bf16 v[40:43], a[104:107], a[88:91], v[40:43]\n"
    "v_mfma_f32_16x16x32_bf16 v[40:43], a[108:111], a[92:95], v[40:43]\n"
    "v_mfma_f32_16x16x32_bf16 v[44:47], a[112:115], a[80:83], v[68:71]\n"
    "v_mfma_f32_16x16x32_bf16 v[44:47], a[116:119], a[84:87], v[44:47]\n"
    "v_mfma_f32_16x16x32_bf16 v[44:47], a[120:123], a[88:91], v[44:47]\n"
    "v_mfma_f32_16x16x32_bf16 v[44:47], a[124:127], a[92:95], v[44:47]\n"
    "v_max3_f32 v121, v0, v1, v2\n"
    "v_max3_f32 v121, v121, v3, v4\n"
    "v_max3_f32 v121, v121, v5, v6\n"
    "v_max_f32 v121, v121, v7\n"
    "v_mov_b32 v122, v121\n"
    "s_nop 1\n"
    "v_permlane16_swap_b32 v121, v122\n"
    "v_max_f32 v121, v121, v122\n"
    "v_mov_b32 v122, v121\n"
    "s_nop 1\n"
    "v_permlane32_swap_b32 v121, v122\n"
    "v_max_f32 v121, v121, v122\n"
    "s_mov_b32 s57, 0xff61b1e6\n"
    "ds_write_b32 v86, v121\n"
    "s_waitcnt lgkmcnt(0)\n"
    "v_max3_f32 v121, v8, v9, v10\n"
    "v_max3_f32 v121, v121, v11, v12\n"
    "v_max3_f32 v121, v121, v13, v14\n"
    "v_max_f32 v121, v121, v15\n"
    "v_mov_b32 v122, v121\n"
    "s_nop 1\n"
    "v_permlane16_swap_b32 v121, v122\n"
    "v_max_f32 v121, v121, v122\n"
    "v_mov_b32 v122, v121\n"
    "s_nop 1\n"
    "v_permlane32_swap_b32 v121, v122\n"
    "v_max_f32 v121, v121, v122\n"
    "ds_write_b32 v86, v121 offset:64\n"
    "s_waitcnt lgkmcnt(0)\n"
    "v_max3_f32 v121, v16, v17, v18\n"
    "v_max3_f32 v121, v121, v19, v20\n"
    "v_max3_f32 v121, v121, v21, v22\n"
    "v_max_f32 v121, v121, v23\n"
    "v_mov_b32 v122, v121\n"
    "s_nop 1\n"
    "v_permlane16_swap_b32 v121, v122\n"
    "v_max_f32 v121, v121, v122\n"
    "v_mov_b32 v122, v121\n"
    "s_nop 1\n"
    "v_permlane32_swap_b32 v121, v122\n"
    "v_max_f32 v121, v121, v122\n"
    "ds_write_b32 v86, v121 offset:128\n"
    "s_waitcnt lgkmcnt(0)\n"
    "v_max3_f32 v121, v24, v25, v26\n"
    "v_max3_f32 v121, v121, v27, v28\n"
    "v_max3_f32 v121, v121, v29, v30\n"
    "v_max_f32 v121, v121, v31\n"
    "v_mov_b32 v122, v121\n"
    "s_nop 1\n"
    "v_permlane16_swap_b32 v121, v122\n"
    "v_max_f32 v121, v121, v122\n"
    "v_mov_b32 v122, v121\n"
    "s_nop 1\n"
    "v_permlane32_swap_b32 v121, v122\n"
    "v_max_f32 v121, v121, v122\n"
    "s_cmp_eq_u32 s92, 0\n"
    "s_cselect_b32 s56, 0x7f7fffff, s57\n"
    "v_min_f32 v121, s56, v121\n"
    "ds_write_b32 v86, v121 offset:192\n"
    "s_waitcnt lgkmcnt(0)\n"
    "v_max3_f32 v121, v32, v33, v34\n"
    "v_max3_f32 v121, v121, v35, v36\n"
    "v_max3_f32 v121, v121, v37, v38\n"
    "v_max_f32 v121, v121, v39\n"
    "v_mov_b32 v122, v121\n"
    "s_nop 1\n"
    "v_permlane16_swap_b32 v121, v122\n"
    "v_max_f32 v121, v121, v122\n"
    "v_mov_b32 v122, v121\n"
    "s_nop 1\n"
    "v_permlane32_swap_b32 v121, v122\n"
    "v_max_f32 v121, v121, v122\n"
    "s_cmp_eq_u32 s92, 0\n"
    "s_cselect_b32 s56, 0x7f7fffff, s57\n"
    "v_min_f32 v121, s56, v121\n"
    "ds_write_b32 v86, v121 offset:256\n"
    "s_waitcnt lgkmcnt(0)\n"
    "v_max3_f32 v121, v40, v41, v42\n"
    "v_max3_f32 v121, v121, v43, v44\n"
    "v_max3_f32 v121, v121, v45, v46\n"
    "v_max_f32 v121, v121, v47\n"
    "v_mov_b32 v122, v121\n"
    "s_nop 1\n"
    "v_permlane16_swap_b32 v121, v122\n"
    "v_max_f32 v121, v121, v122\n"
    "v_mov_b32 v122, v121\n"
    "s_nop 1\n"
    "v_permlane32_swap_b32 v121, v122\n"
    "v_max_f32 v121, v121, v122\n"
    "s_cmp_eq_u32 s92, 0\n"
    "s_cselect_b32 s56, 0x7f7fffff, s57\n"
    "v_min_f32 v121, s56, v121\n"
    "ds_write_b32 v86, v121 offset:320\n"
    "s_waitcnt lgkmcnt(0)\n"
    "s_barrier\n"
    "v_mov_b32 v120, 0\n"
    "ds_write_b32 v81, v120\n"
    "v_and_b32 v88, 15, v82\n"
    "v_lshlrev_b32 v88, 2, v88\n"
    "v_add_u32 v88, 0x26000, v88\n"
    "ds_read_b32 v123, v88\n"
    "ds_read_b32 v124, v88 offset:384\n"
    "ds_read_b32 v125, v88 offset:768\n"
    "ds_read_b32 v126, v88 offset:1152\n"
    "s_waitcnt lgkmcnt(0)\n"
    "v_max3_f32 v121, v123, v124, v125\n"
    "v_max_f32 v121, v121, v126\n"
    "v_add_f32 v121, 0x41c00000, v121\n"
    "v_max_f32 v121, s75, v121\n"
    "v_exp_f32 v122, -v121\n"
    "s_nop 0\n"
    "v_min_f32 v122, 1.0, v122\n"
    "v_mul_f32 v96, v96, v122\n"
    "v_sub_f32 v0, v0, v121\n"
    "v_sub_f32 v1, v1, v121\n"
    "v_sub_f32 v2, v2, v121\n"
    "v_sub_f32 v3, v3, v121\n"
    "v_sub_f32 v4, v4, v121\n"
    "v_sub_f32 v5, v5, v121\n"
    "v_sub_f32 v6, v6, v121\n"
    "v_sub_f32 v7, v7, v121\n"
    "v_sub_f32 v48, v48, v121\n"
    "v_sub_f32 v49, v49, v121\n"
    "v_sub_f32 v50, v50, v121\n"
    "v_sub_f32 v51, v51, v121\n"
    "ds_read_b32 v123, v88 offset:64\n"
    "ds_read_b32 v124, v88 offset:448\n"
    "ds_read_b32 v125, v88 offset:832\n"
    "ds_read_b32 v126, v88 offset:1216\n"
    "s_waitcnt lgkmcnt(0)\n"
    "v_max3_f32 v121, v123, v124, v125\n"
    "v_max_f32 v121, v121, v126\n"
    "v_add_f32 v121, 0x41c00000, v121\n"
    "v_max_f32 v121, s75, v121\n"
    "v_exp_f32 v122, -v121\n"
    "s_nop 0\n"
    "v_min_f32 v122, 1.0, v122\n"
    "v_mul_f32 v100, v100, v122\n"
    "v_sub_f32 v8, v8, v121\n"
    "v_sub_f32 v9, v9, v121\n"
    "v_sub_f32 v10, v10, v121\n"
    "v_sub_f32 v11, v11, v121\n"
    "v_sub_f32 v12, v12, v121\n"
    "v_sub_f32 v13, v13, v121\n"
    "v_sub_f32 v14, v14, v121\n"
    "v_sub_f32 v15, v15, v121\n"
    "v_sub_f32 v52, v52, v121\n"
    "v_sub_f32 v53, v53, v121\n"
    "v_sub_f32 v54, v54, v121\n"
    "v_sub_f32 v55, v55, v121\n"
    "ds_read_b32 v123, v88 offset:128\n"
    "ds_read_b32 v124, v88 offset:512\n"
    "ds_read_b32 v125, v88 offset:896\n"
    "ds_read_b32 v126, v88 offset:1280\n"
    "s_waitcnt lgkmcnt(0)\n"
    "v_max3_f32 v121, v123, v124, v125\n"
    "v_max_f32 v121, v121, v126\n"
    "v_add_f32 v121, 0x41c00000, v121\n"
    "v_max_f32 v121, s75, v121\n"
    "v_exp_f32 v122, -v121\n"
    "s_nop 0\n"
    "v_min_f32 v122, 1.0, v122\n"
    "v_mul_f32 v104, v104, v122\n"
    "v_sub_f32 v16, v16, v121\n"
    "v_sub_f32 v17, v17, v121\n"
    "v_sub_f32 v18, v18, v121\n"
    "v_sub_f32 v19, v19, v121\n"
    "v_sub_f32 v20, v20, v121\n"
    "v_sub_f32 v21, v21, v121\n"
    "v_sub_f32 v22, v22, v121\n"
    "v_sub_f32 v23, v23, v121\n"
    "v_sub_f32 v56, v56, v121\n"
    "v_sub_f32 v57, v57, v121\n"
    "v_sub_f32 v58, v58, v121\n"
    "v_sub_f32 v59, v59, v121\n"
    "ds_read_b32 v123, v88 offset:192\n"
    "ds_read_b32 v124, v88 offset:576\n"
    "ds_read_b32 v125, v88 offset:960\n"
    "ds_read_b32 v126, v88 offset:1344\n"
    "s_waitcnt lgkmcnt(0)\n"
    "v_max3_f32 v121, v123, v124, v125\n"
    "v_max_f32 v121, v121, v126\n"
    "v_add_f32 v121, 0x41c00000, v121\n"
    "v_max_f32 v121, s75, v121\n"
    "v_exp_f32 v122, -v121\n"
    "s_nop 0\n"
    "v_min_f32 v122, 1.0, v122\n"
    "v_mul_f32 v108, v108, v122\n"
    "v_sub_f32 v24, v24, v121\n"
    "v_sub_f32 v25, v25, v121\n"
    "v_sub_f32 v26, v26, v121\n"
    "v_sub_f32 v27, v27, v121\n"
    "v_sub_f32 v28, v28, v121\n"
    "v_sub_f32 v29, v29, v121\n"
    "v_sub_f32 v30, v30, v121\n"
    "v_sub_f32 v31, v31, v121\n"
    "v_sub_f32 v60, v60, v121\n"
    "v_sub_f32 v61, v61, v121\n"
    "v_sub_f32 v62, v62, v121\n"
    "v_sub_f32 v63, v63, v121\n"
    "ds_read_b32 v123, v88 offset:256\n"
    "ds_read_b32 v124, v88 offset:640\n"
    "ds_read_b32 v125, v88 offset:1024\n"
    "ds_read_b32 v126, v88 offset:1408\n"
    "s_waitcnt lgkmcnt(0)\n"
    "v_max3_f32 v121, v123, v124, v125\n"
    "v_max_f32 v121, v121, v126\n"
    "v_add_f32 v121, 0x41c00000, v121\n"
    "v_max_f32 v121, s75, v121\n"
    "v_exp_f32 v122, -v121\n"
    "s_nop 0\n"
    "v_min_f32 v122, 1.0, v122\n"
    "v_mul_f32 v112, v112, v122\n"
    "v_sub_f32 v32, v32, v121\n"
    "v_sub_f32 v33, v33, v121\n"
    "v_sub_f32 v34, v34, v121\n"
    "v_sub_f32 v35, v35, v121\n"
    "v_sub_f32 v36, v36, v121\n"
    "v_sub_f32 v37, v37, v121\n"
    "v_sub_f32 v38, v38, v121\n"
    "v_sub_f32 v39, v39, v121\n"
    "v_sub_f32 v64, v64, v121\n"
    "v_sub_f32 v65, v65, v121\n"
    "v_sub_f32 v66, v66, v121\n"
    "v_sub_f32 v67, v67, v121\n"
    "ds_read_b32 v123, v88 offset:320\n"
    "ds_read_b32 v124, v88 offset:704\n"
    "ds_read_b32 v125, v88 offset:1088\n"
    "ds_read_b32 v126, v88 offset:1472\n"
    "s_waitcnt lgkmcnt(0)\n"
    "v_max3_f32 v121, v123, v124, v125\n"
    "v_max_f32 v121, v121, v126\n"
    "v_add_f32 v121, 0x41c00000, v121\n"
    "v_max_f32 v121, s75, v121\n"
    "v_exp_f32 v122, -v121\n"
    "s_nop 0\n"
    "v_min_f32 v122, 1.0, v122\n"
    "v_mul_f32 v116, v116, v122\n"
    "v_sub_f32 v40, v40, v121\n"
    "v_sub_f32 v41, v41, v121\n"
    "v_sub_f32 v42, v42, v121\n"
    "v_sub_f32 v43, v43, v121\n"
    "v_sub_f32 v44, v44, v121\n"
    "v_sub_f32 v45, v45, v121\n"
    "v_sub_f32 v46, v46, v121\n"
    "v_sub_f32 v47, v47, v121\n"
    "v_sub_f32 v68, v68, v121\n"
    "v_sub_f32 v69, v69, v121\n"
    "v_sub_f32 v70, v70, v121\n"
    "v_sub_f32 v71, v71, v121\n"
    "v_exp_f32 v0, v0\n"
    "v_exp_f32 v1, v1\n"
    "v_exp_f32 v2, v2\n"
    "v_exp_f32 v3, v3\n"
    "v_cvt_pk_bf16_f32 v0, v0, v1\n"
    "v_exp_f32 v4, v4\n"
    "v_exp_f32 v5, v5\n"
    "v_cvt_pk_bf16_f32 v1, v2, v3\n"
    "v_exp_f32 v6, v6\n"
    "v_exp_f32 v7, v7\n"
    "v_cvt_pk_bf16_f32 v2, v4, v5\n"
    "v_cvt_pk_bf16_f32 v3, v6, v7\n"
    "ds_write_b128 v84, v[0:3]\n"
    "v_exp_f32 v8, v8\n"
    "v_exp_f32 v9, v9\n"
    "v_exp_f32 v10, v10\n"
    "v_exp_f32 v11, v11\n"
    "v_cvt_pk_bf16_f32 v8, v8, v9\n"
    "v_exp_f32 v12, v12\n"
    "v_exp_f32 v13, v13\n"
    "v_cvt_pk_bf16_f32 v9, v10, v11\n"
    "v_exp_f32 v14, v14\n"
    "v_exp_f32 v15, v15\n"
    "v_cvt_pk_bf16_f32 v10, v12, v13\n"
    "v_cvt_pk_bf16_f32 v11, v14, v15\n"
    "ds_write_b128 v84, v[8:11] offset:1024\n"
    "v_exp_f32 v16, v16\n"
    "v_exp_f32 v17, v17\n"
    "v_exp_f32 v18, v18\n"
    "v_exp_f32 v19, v19\n"
    "v_cvt_pk_bf16_f32 v16, v16, v17\n"
    "v_exp_f32 v20, v20\n"
    "v_exp_f32 v21, v21\n"
    "v_cvt_pk_bf16_f32 v17, v18, v19\n"
    "v_exp_f32 v22, v22\n"
    "v_exp_f32 v23, v23\n"
    "v_cvt_pk_bf16_f32 v18, v20, v21\n"
    "v_cvt_pk_bf16_f32 v19, v22, v23\n"
    "ds_write_b128 v84, v[16:19] offset:2048\n"
    "v_exp_f32 v24, v24\n"
    "v_exp_f32 v25, v25\n"
    "v_exp_f32 v26, v26\n"
    "v_exp_f32 v27, v27\n"
    "v_cvt_pk_bf16_f32 v24, v24, v25\n"
    "v_exp_f32 v28, v28\n"
    "v_exp_f32 v29, v29\n"
    "v_cvt_pk_bf16_f32 v25, v26, v27\n"
    "v_exp_f32 v30, v30\n"
    "v_exp_f32 v31, v31\n"
    "v_cvt_pk_bf16_f32 v26, v28, v29\n"
    "v_cvt_pk_bf16_f32 v27, v30, v31\n"
    "ds_write_b128 v84, v[24:27] offset:3072\n"
    "s_cmp_eq_u32 s92, 2\n"
    "s_cbranch_scc0 SRZ%=\n"
    "v_mfma_f32_16x16x32_bf16 v[4:7], v[72:75], v[0:3], v[96:99]\n"
    "v_mfma_f32_16x16x32_bf16 v[12:15], v[72:75], v[8:11], v[100:103]\n"
    "v_mfma_f32_16x16x32_bf16 v[20:23], v[72:75], v[16:19], v[104:107]\n"
    "s_nop 5\n"
    "ds_write_b32 v85, v4\n"
    "ds_write_b32 v85, v12 offset:256\n"
    "ds_write_b32 v85, v20 offset:512\n"
    "SRZ%=:\n"
    "s_mov_b32 s75, 0\n"
    "s_waitcnt lgkmcnt(0)\n"
    "v_xor_b32 v83, 0x8000, v83\n"
    "ds_read_b128 a[96:99], v83\n"
    "ds_read_b128 a[100:103], v83 offset:512\n"
    "ds_read_b128 a[104:107], v83 offset:1024\n"
    "ds_read_b128 a[108:111], v83 offset:1536\n"
    "ds_read_b128 a[112:115], v83 offset:4096\n"
    "ds_read_b128 a[116:119], v83 offset:4608\n"
    "ds_read_b128 a[120:123], v83 offset:5120\n"
    "ds_read_b128 a[124:127], v83 offset:5632\n"
    "v_xor_b32 v83, 0x8000, v83\n"
    "s_waitcnt lgkmcnt(0)\n"
    "s_barrier\n"
    "s_setpc_b64 s[30:31]\n"
    "OREDO%=:\n"
    "s_waitcnt lgkmcnt(0)\n"
    "s_nop 15\n"
    "s_barrier\n"
    "v_and_b32 v62, 15, v82\n"
    "v_lshlrev_b32 v62, 2, v62\n"
    "v_add_u32 v62, 0x26000, v62\n"
    "ds_read_b32 v48, v62\n"
    "ds_read_b32 v49, v62 offset:384\n"
    "ds_read_b32 v50, v62 offset:768\n"
    "ds_read_b32 v51, v62 offset:1152\n"
    "s_waitcnt lgkmcnt(0)\n"
    "v_max3_f32 v60, v48, v49, v50\n"
    "v_max_f32 v60, v60, v51\n"
    "v_add_f32 v60, 0x41c00000, v60\n"
    "v_max_f32 v60, s75, v60\n"
    "v_exp_f32 v61, -v60\n"
    "s_nop 0\n"
    "v_min_f32 v61, 1.0, v61\n"
    "v_add_f32 v64, v64, v60\n"
    "v_mul_f32 v0, v0, v61\n"
    "v_mul_f32 v1, v1, v61\n"
    "v_mul_f32 v2, v2, v61\n"
    "v_mul_f32 v3, v3, v61\n"
    "v_mul_f32 v4, v4, v61\n"
    "v_mul_f32 v5, v5, v61\n"
    "v_mul_f32 v6, v6, v61\n"
    "v_mul_f32 v7, v7, v61\n"
    "ds_read_b32 v48, v62 offset:64\n"
    "ds_read_b32 v49, v62 offset:448\n"
    "ds_read_b32 v50, v62 offset:832\n"
    "ds_read_b32 v51, v62 offset:1216\n"
    "s_waitcnt lgkmcnt(0)\n"
    "v_max3_f32 v60, v48, v49, v50\n"
    "v_max_f32 v60, v60, v51\n"
    "v_add_f32 v60, 0x41c00000, v60\n"
    "v_max_f32 v60, s75, v60\n"
    "v_exp_f32 v61, -v60\n"
    "s_nop 0\n"
    "v_min_f32 v61, 1.0, v61\n"
    "v_add_f32 v65, v65, v60\n"
    "v_mul_f32 v8, v8, v61\n"
    "v_mul_f32 v9, v9, v61\n"
    "v_mul_f32 v10, v10, v61\n"
    "v_mul_f32 v11, v11, v61\n"
    "v_mul_f32 v12, v12, v61\n"
    "v_mul_f32 v13, v13, v61\n"
    "v_mul_f32 v14, v14, v61\n"
    "v_mul_f32 v15, v15, v61\n"
    "ds_read_b32 v48, v62 offset:128\n"
    "ds_read_b32 v49, v62 offset:512\n"
    "ds_read_b32 v50, v62 offset:896\n"
    "ds_read_b32 v51, v62 offset:1280\n"
    "s_waitcnt lgkmcnt(0)\n"
    "v_max3_f32 v60, v48, v49, v50\n"
    "v_max_f32 v60, v60, v51\n"
    "v_add_f32 v60, 0x41c00000, v60\n"
    "v_max_f32 v60, s75, v60\n"
    "v_exp_f32 v61, -v60\n"
    "s_nop 0\n"
    "v_min_f32 v61, 1.0, v61\n"
    "v_add_f32 v66, v66, v60\n"
    "v_mul_f32 v16, v16, v61\n"
    "v_mul_f32 v17, v17, v61\n"
    "v_mul_f32 v18, v18, v61\n"
    "v_mul_f32 v19, v19, v61\n"
    "v_mul_f32 v20, v20, v61\n"
    "v_mul_f32 v21, v21, v61\n"
    "v_mul_f32 v22, v22, v61\n"
    "v_mul_f32 v23, v23, v61\n"
    "ds_read_b32 v48, v62 offset:192\n"
    "ds_read_b32 v49, v62 offset:576\n"
    "ds_read_b32 v50, v62 offset:960\n"
    "ds_read_b32 v51, v62 offset:1344\n"
    "s_waitcnt lgkmcnt(0)\n"
    "v_max3_f32 v60, v48, v49, v50\n"
    "v_max_f32 v60, v60, v51\n"
    "v_add_f32 v60, 0x41c00000, v60\n"
    "v_max_f32 v60, s75, v60\n"
    "v_exp_f32 v61, -v60\n"
    "s_nop 0\n"
    "v_min_f32 v61, 1.0, v61\n"
    "v_add_f32 v67, v67, v60\n"
    "v_mul_f32 v24, v24, v61\n"
    "v_mul_f32 v25, v25, v61\n"
    "v_mul_f32 v26, v26, v61\n"
    "v_mul_f32 v27, v27, v61\n"
    "v_mul_f32 v28, v28, v61\n"
    "v_mul_f32 v29, v29, v61\n"
    "v_mul_f32 v30, v30, v61\n"
    "v_mul_f32 v31, v31, v61\n"
    "ds_read_b32 v48, v62 offset:256\n"
    "ds_read_b32 v49, v62 offset:640\n"
    "ds_read_b32 v50, v62 offset:1024\n"
    "ds_read_b32 v51, v62 offset:1408\n"
    "s_waitcnt lgkmcnt(0)\n"
    "v_max3_f32 v60, v48, v49, v50\n"
    "v_max_f32 v60, v60, v51\n"
    "v_add_f32 v60, 0x41c00000, v60\n"
    "v_max_f32 v60, s75, v60\n"
    "v_exp_f32 v61, -v60\n"
    "s_nop 0\n"
    "v_min_f32 v61, 1.0, v61\n"
    "v_add_f32 v68, v68, v60\n"
    "v_mul_f32 v32, v32, v61\n"
    "v_mul_f32 v33, v33, v61\n"
    "v_mul_f32 v34, v34, v61\n"
    "v_mul_f32 v35, v35, v61\n"
    "v_mul_f32 v36, v36, v61\n"
    "v_mul_f32 v37, v37, v61\n"
    "v_mul_f32 v38, v38, v61\n"
    "v_mul_f32 v39, v39, v61\n"
    "ds_read_b32 v48, v62 offset:320\n"
    "ds_read_b32 v49, v62 offset:704\n"
    "ds_read_b32 v50, v62 offset:1088\n"
    "ds_read_b32 v51, v62 offset:1472\n"
    "s_waitcnt lgkmcnt(0)\n"
    "v_max3_f32 v60, v48, v49, v50\n"
    "v_max_f32 v60, v60, v51\n"
    "v_add_f32 v60, 0x41c00000, v60\n"
    "v_max_f32 v60, s75, v60\n"
    "v_exp_f32 v61, -v60\n"
    "s_nop 0\n"
    "v_min_f32 v61, 1.0, v61\n"
    "v_add_f32 v69, v69, v60\n"
    "v_mul_f32 v40, v40, v61\n"
    "v_mul_f32 v41, v41, v61\n"
    "v_mul_f32 v42, v42, v61\n"
    "v_mul_f32 v43, v43, v61\n"
    "v_mul_f32 v44, v44, v61\n"
    "v_mul_f32 v45, v45, v61\n"
    "v_mul_f32 v46, v46, v61\n"
    "v_mul_f32 v47, v47, v61\n"
    "s_mov_b32 s75, 0\n"
    "s_waitcnt lgkmcnt(0)\n"
    "s_barrier\n"
    "s_setpc_b64 s[30:31]\n"
    "CHK2%=:\n"
    "s_waitcnt vmcnt(0)\n"
    "v_cmp_ne_u32 vcc, s27, v91\n"
    "s_cbranch_vccnz SPIN%=\n"
    "SPINX%=:\n"
    "buffer_load_dwordx4 v[108:111], v87, s[80:83], s79 offen offset:3072 sc1\n"
    "buffer_load_dwordx4 v[96:99], v87, s[80:83], s79 offen sc1\n"
    "buffer_load_dwordx4 v[100:103], v87, s[80:83], s79 offen offset:1024 sc1\n"
    "buffer_load_dwordx4 v[104:107], v87, s[80:83], s79 offen offset:2048 sc1\n"
    "s_add_u32 s57, s92, 0x20000\n"
    "buffer_load_dword v92, off, s[80:83], s57 sc1\n"
    "s_mov_b32 s90, 1\n"
    "s_branch CHKX%=\n"
    "SPIN%=:\n"
    "s_mov_b32 s56, 0\n"
    "s_add_u32 s57, s92, 0x20000\n"
    "SPINL%=:\n"
    "buffer_load_dword v91, off, s[80:83], s91 sc1\n"
    "buffer_load_dword v92, off, s[80:83], s57 sc1\n"
    "s_waitcnt vmcnt(0)\n"
    "v_cmp_eq_u32 vcc, s27, v91\n"
    "s_cbranch_vccnz SPINX%=\n"
    "v_cmp_eq_u32 vcc, s28, v92\n"
    "s_cbranch_vccnz SPINN%=\n"
    "v_readfirstlane_b32 s28, v92\n"
    "buffer_store_dword v92, v90, s[80:83], s92 offen sc1\n"
    "SPINN%=:\n"
    "s_sleep 1\n"
    "s_add_u32 s56, s56, 1\n"
    "s_cmp_lt_u32 s56, 0x200000\n"
    "s_cbranch_scc1 SPINL%=\n"
    "s_branch SPINX%=\n"
    :: "s"(__builtin_amdgcn_kernarg_segment_ptr()), "s"(blockIdx.x), "s"(w)
    : "v0", "v1", "v2", "v3", "v4", "v5", "v6", "v7", "v8", "v9", "v10", "v11", "v12", "v13", "v14", "v15", "v16", "v17", "v18", "v19", "v20", "v21", "v22", "v23", "v24", "v25", "v26", "v27", "v28", "v29", "v30", "v31", "v32", "v33", "v34", "v35", "v36", "v37", "v38", "v39", "v40", "v41", "v42", "v43", "v44", "v45", "v46", "v47", "v48", "v49", "v50", "v51", "v52", "v53", "v54", "v55", "v56", "v57", "v58", "v59", "v60", "v61", "v62", "v63", "v64", "v65", "v66", "v67", "v68", "v69", "v70", "v71", "v72", "v73", "v74", "v75", "v76", "v77", "v78", "v79", "v80", "v81", "v82", "v83", "v84", "v85", "v86", "v87", "v88", "v89", "v90", "v91", "v92", "v93", "v94", "v95", "v96", "v97", "v98", "v99", "v100", "v101", "v102", "v103", "v104", "v105", "v106", "v107", "v108", "v109", "v110", "v111", "v112", "v113", "v114", "v115", "v116", "v117", "v118", "v119", "v120", "v121", "v122", "v123", "v124", "v125", "v126", "v127", "a0", "a1", "a2", "a3", "a4", "a5", "a6", "a7", "a8", "a9", "a10", "a11", "a12", "a13", "a14", "a15", "a16", "a17", "a18", "a19", "a20", "a21", "a22", "a23", "a24", "a25", "a26", "a27", "a28", "a29", "a30", "a31", "a32", "a33", "a34", "a35", "a36", "a37", "a38", "a39", "a40", "a41", "a42", "a43", "a44", "a45", "a46", "a47", "a48", "a49", "a50", "a51", "a52", "a53", "a54", "a55", "a56", "a57", "a58", "a59", "a60", "a61", "a62", "a63", "a64", "a65", "a66", "a67", "a68", "a69", "a70", "a71", "a72", "a73", "a74", "a75", "a76", "a77", "a78", "a79", "a80", "a81", "a82", "a83", "a84", "a85", "a86", "a87", "a88", "a89", "a90", "a91", "a92", "a93", "a94", "a95", "a96", "a97", "a98", "a99", "a100", "a101", "a102", "a103", "a104", "a105", "a106", "a107", "a108", "a109", "a110", "a111", "a112", "a113", "a114", "a115", "a116", "a117", "a118", "a119", "a120", "a121", "a122", "a123", "a124", "a125", "a126", "a127", "s20", "s21", "s22", "s23", "s24", "s25", "s26", "s27", "s28", "s29", "s30", "s31", "s36", "s37", "s38", "s39", "s40", "s41", "s42", "s43", "s44", "s45", "s46", "s47", "s48", "s49", "s50", "s51", "s52", "s53", "s54", "s55", "s56", "s57", "s58", "s59", "s60", "s61", "s62", "s63", "s64", "s65", "s66", "s67", "s68", "s69", "s70", "s71", "s72", "s73", "s74", "s75", "s76", "s77", "s78", "s79", "s80", "s81", "s82", "s83", "s84", "s85", "s86", "s87", "s88", "s89", "s90", "s91", "s92", "s93", "s94", "s95", "s96", "s97", "s98", "s99", "vcc", "scc", "memory");
}

extern "C" void kernel_launch(void* const* d_in, const int* in_sizes, int n_in, void* d_out, int out_size, void* d_ws, size_t ws_size, hipStream_t stream) {
  (void)in_sizes; (void)n_in; (void)out_size; (void)ws_size;
  Args a;
  a.k.p = d_in[1]; a.k.bytes = 33554432u; a.k.fmt = 0x20000u;
  a.v.p = d_in[2]; a.v.bytes = 33554432u; a.v.fmt = 0x20000u;
  a.q.p = d_in[0]; a.q.bytes = 3145728u;  a.q.fmt = 0x20000u;
  a.o.p = d_out;   a.o.bytes = 3145728u;  a.o.fmt = 0x20000u;
  a.ws.p = d_ws;   a.ws.bytes = 5242880u; a.ws.fmt = 0x20000u;
  (void)hipFuncSetAttribute((const void*)gqa_fwd, hipFuncAttributeMaxDynamicSharedMemorySize, 158848);
  hipLaunchKernelGGL(gqa_fwd, dim3(256), dim3(512), 158848, stream, a);
}
```
